```python
import torch

def _softmax(x, axis=-1):
    return torch.softmax(x, dim=axis)
def _gelu(x):
    return torch.nn.functional.gelu(x, approximate="tanh")
def _silu(x):
    return torch.nn.functional.silu(x)
def _sigmoid(x):
    return torch.sigmoid(x)
def _layernorm(x, axis=-1, eps=1e-5):
    m = x.mean(dim=axis, keepdim=True)
    v = x.var(dim=axis, keepdim=True, unbiased=False)
    return (x - m) / torch.sqrt(v + eps)
def _scatter_add(tgt, idx, src, axis=0):
    return tgt.clone().index_add_(axis, idx, src)
def _segment_sum(src, seg, n):
    out = torch.zeros((n,) + src.shape[1:], dtype=src.dtype)
    return out.index_add_(0, seg, src)
def _pad(x, pw):
    flat = tuple(v for pair in reversed(pw) for v in pair)
    return torch.nn.functional.pad(x, flat)

kernel_name = 'gated_mix__sliding_local_dilated__softmax_full_mha__prompt'

def setup_inputs():

    _w = (lambda a: (lambda t: t.to(torch.bfloat16) if t.is_floating_point() else t)(torch.as_tensor(a)))
    def _A_setup():
        B, S, D = 1, 2048, 640
        return {
            "x": _w((lambda *s: torch.randn(*s, dtype=torch.bfloat16) * 0.05)(B, S, D)),
            "wqkv": _w((lambda *s: torch.randn(*s, dtype=torch.bfloat16) * 1.6)(D, 3 * D)),
            "wo": _w((lambda *s: torch.randn(*s, dtype=torch.bfloat16) * 0.05)(D, D)),
        }
    _A_in = _A_setup()

    def _B_setup():
        B, S, D = 1, 2048, 640
        return {
            "x": _w((lambda *s: torch.randn(*s, dtype=torch.bfloat16) * 0.05)(B, S, D)),
            "wqkv": _w((lambda *s: torch.randn(*s, dtype=torch.bfloat16) * 1.6)(D, 3 * D)),
            "wo": _w((lambda *s: torch.randn(*s, dtype=torch.bfloat16) * 0.1)(D, D)),
        }
    _B_in = _B_setup()

    return {
        "A_x": _A_in["x"],
        "A_wqkv": _A_in["wqkv"],
        "A_wo": _A_in["wo"],
        "B_x": _B_in["x"],
        "B_wqkv": _B_in["wqkv"],
        "B_wo": _B_in["wo"],
        "gate_w": _w((lambda *s: torch.randn(*s, dtype=torch.bfloat16) * 0.05)(640) * 16.0),
    }

def reference(A_x, A_wqkv, A_wo, B_x, B_wqkv, B_wo, gate_w):
    def _A_ref(x, wqkv, wo):
        B, S, D = x.shape
        H = 32; Dh = D // H
        W, dil = 8, 2
        qkv = (lambda x, *d: x.permute(*d))((x @ wqkv).reshape(B, S, 3, H, Dh), 2, 0, 3, 1, 4)
        q, k, v = qkv[0], qkv[1], qkv[2]
        scale = Dh ** -0.5
        pos = torch.arange(S)
        off = torch.arange(W) * dil
        idx = pos[:, None] - off[None, :]
        mask = idx >= 0
        idx_c = torch.where(mask, idx, 0).reshape(-1)
        kg = k[:, :, idx_c, :].reshape(B, H, S, W, Dh)
        vg = v[:, :, idx_c, :].reshape(B, H, S, W, Dh)
        logits = torch.einsum("bhsd,bhswd->bhsw", q, kg) * scale
        logits = torch.where(mask[None, None, :, :], logits, -1e9)
        attn = _softmax(logits, axis=-1)
        o = torch.einsum("bhsw,bhswd->bhsd", attn, vg)
        return (lambda x, *d: x.permute(*d))(o, 0, 2, 1, 3).reshape(B, S, D) @ wo

    def _B_ref(x, wqkv, wo):
        B, S, D = x.shape
        H = 32; Dh = D // H
        qkv = (lambda x, *d: x.permute(*d))((x @ wqkv).reshape(B, S, 3, H, Dh), 2, 0, 3, 1, 4)
        q, k, v = qkv[0], qkv[1], qkv[2]
        scale = Dh ** -0.5
        scores = torch.einsum("bhqd,bhkd->bhqk", q, k) * scale
        p = _softmax(scores, axis=-1)
        o = torch.einsum("bhqk,bhkd->bhqd", p, v)
        return (lambda x, *d: x.permute(*d))(o, 0, 2, 1, 3).reshape(B, S, D) @ wo

    x = A_x
    g = _sigmoid((x * gate_w).sum(-1, keepdims=True))
    a = _A_ref(x, A_wqkv, A_wo)
    b = _B_ref(x, B_wqkv, B_wo)
    return g * a + (1 - g) * b
```

```cpp
#include <hip/hip_runtime.h>
#include <hip/hip_bf16.h>
typedef __hip_bfloat16 bf16;
constexpr int S = 2048, D = 640, H = 32, DH = 20;
__global__ void k_qkv(const bf16* x, const bf16* wa, const bf16* wb, float* qkv) {
    int n = blockIdx.x * blockDim.x + threadIdx.x;
    int r0 = blockIdx.y * 8;
    const bf16* w = n < 1920 ? wa + n : wb + (n - 1920);
    float acc[8] = {};
    for (int k = 0; k < D; ++k) { float wv = __bfloat162float(w[(size_t)k * 1920]);
        for (int i = 0; i < 8; ++i) acc[i] += __bfloat162float(x[(r0 + i) * D + k]) * wv; }
    for (int i = 0; i < 8; ++i) qkv[(size_t)(r0 + i) * 3840 + n] = __bfloat162float(__float2bfloat16(acc[i]));
}
__global__ void k_attn(const float* qkv, float* o) {
    int q = blockIdx.x * blockDim.x + threadIdx.x; int h = blockIdx.y; int br = blockIdx.z;
    const float* base = qkv + br * 1920 + h * DH;
    float qv[DH]; for (int d = 0; d < DH; ++d) qv[d] = base[(size_t)q * 3840 + d] * 0.22360679775f;
    float m = -1e30f, l = 0, acc[DH] = {};
    int n = br == 0 ? 8 : S;
    for (int j = 0; j < n; ++j) { int key = br == 0 ? q - 2 * j : j; if (key < 0) continue;
        const float* kp = base + (size_t)key * 3840 + 640; float s = 0; for (int d = 0; d < DH; ++d) s += qv[d] * kp[d];
        float mn = fmaxf(m, s), a = __expf(m - mn), p = __expf(s - mn); l = l * a + p; for (int d = 0; d < DH; ++d) acc[d] = acc[d] * a + p * kp[640 + d]; m = mn; }
    for (int d = 0; d < DH; ++d) o[(size_t)q * 1280 + br * 640 + h * DH + d] = __bfloat162float(__float2bfloat16(acc[d] / l));
}
__global__ void k_out(const bf16* x, const bf16* gw, const float* o, const bf16* woa, const bf16* wob, bf16* out) {
    int n = blockIdx.x * blockDim.x + threadIdx.x; int r = blockIdx.y;
    float g = 0; for (int k = 0; k < D; ++k) g += __bfloat162float(x[r * D + k]) * __bfloat162float(gw[k]);
    g = 1.f / (1.f + __expf(-g));
    float a = 0, b = 0;
    for (int k = 0; k < D; ++k) { a += o[(size_t)r * 1280 + k] * __bfloat162float(woa[k * D + n]); b += o[(size_t)r * 1280 + 640 + k] * __bfloat162float(wob[k * D + n]); }
    out[r * D + n] = __float2bfloat16(g * a + (1.f - g) * b);
}
extern "C" void kernel_launch(void* const* d_in, const int* in_sizes, int n_in, void* d_out, int out_size, void* d_ws, size_t ws_size, hipStream_t stream) {
    const bf16* x = (const bf16*)d_in[0]; const bf16* wa = (const bf16*)d_in[1]; const bf16* woa = (const bf16*)d_in[2];
    const bf16* wb = (const bf16*)d_in[4]; const bf16* wob = (const bf16*)d_in[5]; const bf16* gw = (const bf16*)d_in[6];
    float* qkv = (float*)d_ws; float* o = qkv + (size_t)S * 3840;
    k_qkv<<<dim3(15, S / 8), 256, 0, stream>>>(x, wa, wb, qkv);
    k_attn<<<dim3(S / 64, H, 2), 64, 0, stream>>>(qkv, o);
    k_out<<<dim3(5, S), 128, 0, stream>>>(x, gw, o, woa, wob, (bf16*)d_out);
}
```

```cpp
#include <hip/hip_runtime.h>
#include <hip/hip_bf16.h>

typedef float f32x32 __attribute__((ext_vector_type(32)));
typedef float f32x16 __attribute__((ext_vector_type(16)));
typedef float f32x8 __attribute__((ext_vector_type(8)));
typedef float f32x4 __attribute__((ext_vector_type(4)));
typedef short s16x4 __attribute__((ext_vector_type(4)));
typedef short s16x8 __attribute__((ext_vector_type(8)));
typedef unsigned u32x2 __attribute__((ext_vector_type(2)));
typedef unsigned u32x4 __attribute__((ext_vector_type(4)));
typedef __bf16 bfv8 __attribute__((ext_vector_type(8)));
typedef __bf16 bfv16 __attribute__((ext_vector_type(16)));
#define LAS __attribute__((address_space(3)))
#define GAS __attribute__((address_space(1)))
typedef LAS const char* lptr;
#define SBAR() __builtin_amdgcn_sched_barrier(0)

constexpr int S = 2048, D = 640;
constexpr float QSCALE = 0.22360679774997896f * 1.4426950408889634f;
constexpr size_t WS_Q = 0;
constexpr size_t Q_BR = 32 * 32 * 2560;
constexpr size_t WS_KV = WS_Q + 2 * Q_BR;
constexpr size_t KV_BR = 32 * 32 * 5120;
constexpr size_t WS_O = WS_KV + 2 * KV_BR;
constexpr size_t O_BR = 32 * 160 * 512;
constexpr size_t WS_G = WS_O + 2 * O_BR;
constexpr size_t WS_WT = WS_G + 8192;
constexpr size_t WT_BR = 40 * 20 * 1024;

__device__ __forceinline__ unsigned cvtpk(float lo, float hi) { unsigned r; asm("v_cvt_pk_bf16_f32 %0, %1, %2" : "=v"(r) : "v"(lo), "v"(hi)); return r; }
__device__ __forceinline__ void glds16(const void* g, unsigned lds_base) {
    unsigned sv; asm volatile("s_mov_b32 %0, m0\n\ts_mov_b32 m0, %2\n\ts_nop 0\n\tglobal_load_lds_dwordx4 %1, off\n\ts_mov_b32 m0, %0" : "=&s"(sv) : "v"(g), "s"(lds_base) : "memory"); }
__device__ __forceinline__ s16x4 ldtr(lptr p) { return __builtin_amdgcn_ds_read_tr16_b64_v4i16((LAS s16x4*)p); }
__device__ __forceinline__ bfv8 cat(s16x4 a, s16x4 b) { return __builtin_bit_cast(bfv8, __builtin_shufflevector(a, b, 0, 1, 2, 3, 4, 5, 6, 7)); }
__device__ __forceinline__ float bf2f(unsigned short v) { return __uint_as_float((unsigned)v << 16); }
#define MFMA32(a, b, c) __builtin_amdgcn_mfma_f32_32x32x16_bf16(a, b, c, 0, 0, 0)

constexpr int K1_XS = 16384, K1_WS = 32768, K1_LDS = 3 * (K1_XS + K1_WS);
__global__ __launch_bounds__(512, 2) void k1_qkv(const char* __restrict__ x, const char* __restrict__ wa, const char* __restrict__ wb,
                                                 const char* __restrict__ woa, const char* __restrict__ wob, const char* __restrict__ gw, char* __restrict__ ws) {
    extern __shared__ __attribute__((aligned(16))) char lds[];
    const int b = blockIdx.x, tid = threadIdx.x, lane = tid & 63, wid = __builtin_amdgcn_readfirstlane(tid >> 6);
    const int r = lane & 31, h2 = lane >> 5;
    if (b >= 240) {
        const int e = b - 240;
        {
            const int row = e * 128 + wid * 16 + (lane >> 2);
            float acc = 0.f;
#pragma unroll 4
            for (int c = 0; c < 20; ++c) {
                const int off = (c * 4 + (lane & 3)) * 16;
                u32x4 xv = *(const u32x4*)(x + (size_t)row * 1280 + off), gv = *(const u32x4*)(gw + off);
#pragma unroll
                for (int i = 0; i < 4; ++i) { acc += __uint_as_float(xv[i] << 16) * __uint_as_float(gv[i] << 16); acc += __uint_as_float(xv[i] & 0xffff0000u) * __uint_as_float(gv[i] & 0xffff0000u); }
            }
            acc += __shfl_xor(acc, 1); acc += __shfl_xor(acc, 2);
            if ((lane & 3) == 0) ((float*)(ws + WS_G))[row] = 1.f / (1.f + __expf(-acc));
        }
        for (int sl = e; sl < 40; sl += 16) {
            const int br = sl / 20, ks = sl % 20;
            const char* src = (br ? wob : woa) + (size_t)ks * 32 * 1280;
            __syncthreads();
#pragma unroll
            for (int i = 0; i < 5; ++i) { const int p = (i * 512 + tid) * 16; *(LAS u32x4*)((LAS char*)lds + p + (p / 1280) * 64) = *(const u32x4*)(src + p); }
            __syncthreads();
#pragma unroll
            for (int i = 0; i < 5; ++i) {
                const int nt = wid * 5 + i;
                lptr p = (lptr)lds + ((lane >> 4) * 8 + ((lane & 15) >> 2)) * 1344 + nt * 32 + (lane & 3) * 8;
                bfv8 f = cat(ldtr(p), ldtr(p + 4 * 1344));
                *(bfv8*)(ws + WS_WT + br * WT_BR + (size_t)(nt * 20 + ks) * 1024 + lane * 16) = f;
            }
        }
        return;
    }
    const int xc = b & 7, t = (xc & 3) * 30 + (b >> 3), nt = t >> 3, mt = (xc >> 2) * 8 + (t & 7);
    const unsigned lbase = (unsigned)(size_t)lds;
    const char* xs[2]; const char* wsrc[4];
#pragma unroll
    for (int i = 0; i < 2; ++i) { const int p = (wid * 2 + i) * 8 + (lane >> 3), T = p ^ ((p >> 5) & 1); xs[i] = x + (size_t)(mt * 128 + T) * 1280 + (((lane & 7) ^ ((p >> 1) & 7)) << 4); }
#pragma unroll
    for (int i = 0; i < 4; ++i) { const int k = (wid * 4 + i) * 2 + h2, c = r ^ ((k & 3) << 2), col = nt * 256 + c * 8;
        wsrc[i] = (col < 1920 ? wa + col * 2 : wb + (col - 1920) * 2) + (size_t)k * 3840; }
#define K1_STAGE(kt, slot) do { \
        glds16(xs[0] + (kt) * 128, lbase + (slot) * K1_XS + wid * 2048); glds16(xs[1] + (kt) * 128, lbase + (slot) * K1_XS + wid * 2048 + 1024); \
        _Pragma("unroll") for (int i_ = 0; i_ < 4; ++i_) glds16(wsrc[i_] + (size_t)(kt) * (64 * 3840), lbase + 3 * K1_XS + (slot) * K1_WS + wid * 4096 + i_ * 1024); } while (0)
    K1_STAGE(0, 0); K1_STAGE(1, 1);
    const int wm = wid & 1, wn = wid >> 1;
    const int col0 = nt * 256 + wn * 64, br = col0 >= 1920, cb = col0 - br * 1920, sec = cb / 640, sc = cb - sec * 640;
    const bool isv = sec == 2;
    int xoff[2];
#pragma unroll
    for (int tt = 0; tt < 2; ++tt) { const int rp = (r & 19) | ((r & 4) << 1) | ((r & 8) >> 1); const int T = isv ? tt * 32 + rp : 2 * r + tt;
        xoff[tt] = (wm * 64 + (T ^ (T >> 5))) * 128 + ((h2 ^ ((T >> 1) & 7)) << 4); }
    const int q4 = (lane & 15) >> 2;
    const int woff = 3 * K1_XS + (h2 * 8 + q4) * 512 + ((wn * 128 + ((lane >> 4) & 1) * 32 + (lane & 3) * 8) ^ (q4 << 6));
    f32x16 acc[2][2] = {};
    int slot = 0;
    for (int kt = 0; kt < 10; ++kt) {
        if (kt < 9) asm volatile("s_waitcnt vmcnt(6)\n\ts_barrier" ::: "memory"); else asm volatile("s_waitcnt vmcnt(0)\n\ts_barrier" ::: "memory");
        if (kt < 8) { const int s2 = slot == 0 ? 2 : slot - 1; K1_STAGE(kt + 2, s2); }
        lptr xb = (lptr)lds + slot * K1_XS, wbp = (lptr)lds + slot * K1_WS;
#pragma unroll
        for (int ks = 0; ks < 4; ++ks) {
            bfv8 xf[2], wf[2];
#pragma unroll
            for (int i = 0; i < 2; ++i) { xf[i] = *(LAS const bfv8*)(xb + (xoff[i] ^ (ks * 32))); lptr p = wbp + (woff ^ (i * 64)) + ks * 8192; wf[i] = cat(ldtr(p), ldtr(p + 2048)); }
            if (isv) { for (int i = 0; i < 2; ++i) for (int j = 0; j < 2; ++j) acc[i][j] = MFMA32(xf[j], wf[i], acc[i][j]); }
            else     { for (int i = 0; i < 2; ++i) for (int j = 0; j < 2; ++j) acc[i][j] = MFMA32(wf[i], xf[j], acc[i][j]); }
        }
        slot = slot == 2 ? 0 : slot + 1;
    }
    const int pair = mt * 2 + wm;
    if (isv) {
#pragma unroll
        for (int ft = 0; ft < 2; ++ft) { const int c = sc + ft * 32 + r, head = c / 20, d = c - head * 20;
            char* dst = ws + WS_KV + br * KV_BR + (size_t)head * 163840 + pair * 5120 + 2560 + h2 * 320 + d * 16;
#pragma unroll
            for (int tt = 0; tt < 2; ++tt)
#pragma unroll
                for (int s = 0; s < 2; ++s) { const f32x16& a = acc[ft][tt];
                    *(u32x4*)(dst + (tt * 4 + s * 2) * 320) = u32x4{cvtpk(a[8 * s], a[8 * s + 1]), cvtpk(a[8 * s + 2], a[8 * s + 3]), cvtpk(a[8 * s + 4], a[8 * s + 5]), cvtpk(a[8 * s + 6], a[8 * s + 7])}; } }
    } else {
        const float sca = sec == 0 ? QSCALE : 1.f;
        char* base = sec == 0 ? ws + WS_Q + br * Q_BR + pair * 2560 : ws + WS_KV + br * KV_BR + pair * 5120;
        const int hs = sec == 0 ? 81920 : 163840;
#pragma unroll
        for (int ft = 0; ft < 2; ++ft)
#pragma unroll
            for (int i = 0; i < 4; ++i) { const int G = (sc >> 2) + ft * 8 + 2 * i + h2, head = G / 5, g = G - head * 5;
                const f32x16 &a = acc[ft][0], &c = acc[ft][1];
                *(u32x4*)(base + (size_t)head * hs + g * 512 + r * 16) = u32x4{cvtpk(a[4 * i] * sca, a[4 * i + 1] * sca), cvtpk(a[4 * i + 2] * sca, a[4 * i + 3] * sca),
                                                                               cvtpk(c[4 * i] * sca, c[4 * i + 1] * sca), cvtpk(c[4 * i + 2] * sca, c[4 * i + 3] * sca)}; }
    }
}

constexpr int K2_CONST = 15 * 10240, K2_LDS = K2_CONST + 3072;
#define EX(s, a) s[a] = __builtin_amdgcn_exp2f(s[a])
#define Q8(s, o) __builtin_bit_cast(bfv16, __builtin_shufflevector(s, s, o, o + 1, o + 2, o + 3, o + 4, o + 5, o + 6, o + 7))
#define LO16(s) __builtin_shufflevector(s, s, 0, 1, 2, 3, 4, 5, 6, 7, 8, 9, 10, 11, 12, 13, 14, 15)
#define HI16(s) __builtin_shufflevector(s, s, 16, 17, 18, 19, 20, 21, 22, 23, 24, 25, 26, 27, 28, 29, 30, 31)
#define SMF(o, v, s, off) o = __builtin_amdgcn_smfmac_f32_32x32x32_bf16(v, Q8(s, off), o, idx, 0, 0)
#define MF2B(a, b, c, cbsz, abid) __builtin_amdgcn_mfma_f32_32x32x4bf16_1k(a, b, c, cbsz, abid, 0)
__device__ __forceinline__ bfv8 ldk(lptr p) { return cat(*(LAS const s16x4*)p, *(LAS const s16x4*)(p + 512)); }
__device__ __forceinline__ bfv8 gldk(const char* p) { return cat(*(const s16x4*)p, *(const s16x4*)(p + 512)); }
__device__ __forceinline__ float swapmax(float m) { auto rr = __builtin_amdgcn_permlane32_swap(__float_as_uint(m), __float_as_uint(m), false, false); return fmaxf(__uint_as_float(rr[0]), __uint_as_float(rr[1])); }
__device__ __forceinline__ float hival(float m) { auto rr = __builtin_amdgcn_permlane32_swap(__float_as_uint(m), __float_as_uint(m), false, false); return __uint_as_float(rr[1]); }
__device__ __forceinline__ float max16(const f32x16& p) { float a = fmaxf(fmaxf(p[0], p[1]), p[2]);
#pragma unroll
    for (int i = 3; i < 15; i += 2) a = fmaxf(fmaxf(a, p[i]), p[i + 1]);
    return fmaxf(a, p[15]); }
__device__ __forceinline__ void store_o(char* dst, const f32x16& o, float sc, int h2) {
    *(u32x2*)(dst + h2 * 512) = u32x2{cvtpk(o[0] * sc, o[1] * sc), cvtpk(o[2] * sc, o[3] * sc)};
    *(u32x2*)(dst + 1024 + h2 * 512) = u32x2{cvtpk(o[4] * sc, o[5] * sc), cvtpk(o[6] * sc, o[7] * sc)};
    if (h2 == 0) *(u32x2*)(dst + 2048) = u32x2{cvtpk(o[8] * sc, o[9] * sc), cvtpk(o[10] * sc, o[11] * sc)};
}

__global__ __launch_bounds__(512, 2) void k2_attn(char* __restrict__ ws) {
    extern __shared__ __attribute__((aligned(16))) char lds[];
    const int b = blockIdx.x, tid = threadIdx.x, lane = tid & 63, wid = __builtin_amdgcn_readfirstlane(tid >> 6);
    const int r = lane & 31, h2 = lane >> 5;
    const int head = (b & 7) * 4 + ((b >> 3) & 3), qb = b >> 5;
    const int qp = wid & 3, kh = wid >> 2;
    const unsigned lbase = (unsigned)(size_t)lds;
    const bfv8 vconst = __builtin_bit_cast(bfv8, r == 20 ? u32x4{0x3f803f80u, 0x3f803f80u, 0x3f803f80u, 0x3f803f80u} : u32x4{0u, 0u, 0u, 0u});
    const int rv = r < 20 ? r : 0;
    int idx = 0xdddd; asm volatile("" : "+v"(idx));
    const int tl = qb * 8 + wid;
    const int tp = tl > 0 ? tl - 1 : 0;
    const char* qa = ws + WS_Q + (size_t)head * 81920 + (tl >> 1) * 2560 + (tl & 1) * 256 + r * 8;
    const char* kd = ws + WS_KV + (size_t)head * 163840 + (tl >> 1) * 5120;
    const char* kpv = ws + WS_KV + (size_t)head * 163840 + (tp >> 1) * 5120;
    const bfv8 aQ = gldk(qa + h2 * 1024); const s16x4 aQr = *(const s16x4*)(qa + 2048);
    const bfv8 aKd = gldk(kd + (tl & 1) * 256 + r * 8 + h2 * 1024), aKp = gldk(kpv + (tp & 1) * 256 + r * 8 + h2 * 1024);
    const s16x4 aKr = *(const s16x4*)((h2 ? kd + (tl & 1) * 256 : kpv + (tp & 1) * 256) + 2048 + r * 8);
    bfv8 aV0 = *(const bfv8*)(kd + 2560 + (tl & 1) * 1280 + h2 * 320 + rv * 16), aV1 = *(const bfv8*)(kd + 2560 + (tl & 1) * 1280 + 640 + h2 * 320 + rv * 16);
    bfv8 aVp = *(const bfv8*)(kpv + 2560 + (tp & 1) * 1280 + 640 + h2 * 320 + rv * 16);
    const float gate_a = ((const float*)(ws + WS_G))[tl * 32 + r];
    const char* qg = ws + WS_Q + Q_BR + (size_t)head * 81920 + (qb * 4 + qp) * 2560;
    const char* kvg = ws + WS_KV + KV_BR + (size_t)head * 163840;
    const char* kv0 = kvg + kh * 81920;
    const bfv8 Q0 = gldk(qg + h2 * 1024 + r * 8), Q1 = gldk(qg + h2 * 1024 + 256 + r * 8); const s16x4 Qr = *(const s16x4*)(qg + 2048 + lane * 8);
    const bfv8 K00 = gldk(kv0 + h2 * 1024 + r * 8); bfv8 Kn = gldk(kv0 + h2 * 1024 + 256 + r * 8); s16x4 Kr = *(const s16x4*)(kv0 + 2048 + lane * 8);
    bfv8 F[4];
#pragma unroll
    for (int i = 0; i < 4; ++i) F[i] = *(const bfv8*)(kv0 + 2560 + i * 640 + h2 * 320 + rv * 16);
    const float gate_b = 1.f - ((const float*)(ws + WS_G))[qb * 256 + qp * 64 + kh * 32 + r];
    if (tid < 192) *(LAS u32x4*)((LAS char*)lds + K2_CONST + tid * 16) = (tid % 20 == 0) ? u32x4{0x3f803f80u, 0x3f803f80u, 0x3f803f80u, 0x3f803f80u} : u32x4{0u, 0u, 0u, 0u};
    asm volatile("s_waitcnt vmcnt(0)" ::: "memory");
#pragma unroll 1
    for (int n = 0; n < 19; ++n) { int p = n * 8 + wid; p = p < 150 ? p : 149; const int it = p / 10, w = p - it * 10, half = w / 5, sub = w - half * 5;
        glds16(kvg + half * 81920 + (it + 1) * 5120 + sub * 1024 + lane * 16, lbase + p * 1024); }
    {
        if (r >= 20) { aV0 = vconst; aV1 = vconst; aVp = vconst; }
        f32x32 t = MF2B(aKr, aQr, f32x32{}, 0, 0);
        f32x16 sp = MFMA32(aKp, aQ, LO16(t)), sd = MFMA32(aKd, aQ, HI16(t));
        const int dl = r - 4 * h2; float m = -3.0e38f;
#pragma unroll
        for (int i = 0; i < 16; ++i) { const int row = (i & 3) + 8 * (i >> 2); const unsigned dd = dl - row; sd[i] = (dd <= 14u && !(dd & 1)) ? sd[i] : -3.0e38f; m = fmaxf(m, sd[i]);
            if (i >= 8) { const unsigned dp = dl - row + 32; sp[i] = (dp <= 14u && !(dp & 1) && tl > 0) ? sp[i] : -3.0e38f; m = fmaxf(m, sp[i]); } }
        m = swapmax(m);
#pragma unroll
        for (int i = 0; i < 16; ++i) { sd[i] = __builtin_amdgcn_exp2f(sd[i] - m); if (i >= 8) sp[i] = __builtin_amdgcn_exp2f(sp[i] - m); }
        f32x16 o = {};
        SMF(o, aVp, sp, 8); SMF(o, aV0, sd, 0); SMF(o, aV1, sd, 8);
        const float sc = gate_a / hival(o[8]);
        store_o(ws + WS_O + (size_t)(tl >> 1) * 81920 + head * 2560 + (tl & 1) * 256 + r * 8, o, sc, h2);
    }
    if (r >= 20) { F[0] = vconst; F[1] = vconst; F[2] = vconst; F[3] = vconst; }
    f32x16 a0, a1, b0 = {}, b1 = {}, o0 = {}, o1 = {}; f32x32 negm; float m0, m1;
    {
        f32x32 t = MF2B(Kr, Qr, f32x32{}, 1, 0);
        a0 = MFMA32(K00, Q0, LO16(t)); a1 = MFMA32(K00, Q1, HI16(t));
        m0 = swapmax(max16(a0)); m1 = swapmax(max16(a1));
#pragma unroll
        for (int i = 0; i < 16; ++i) { negm[i] = -m0; negm[16 + i] = -m1; a0[i] -= m0; a1[i] -= m1; }
    }
    lptr kp = (lptr)lds + kh * 5120 + h2 * 1024 + r * 8, rp = (lptr)lds + kh * 5120 + 2048 + lane * 8;
    lptr vp = (lptr)lds + (r < 20 ? kh * 5120 + 2560 : K2_CONST) + (h2 * 20 + r) * 16;
    const int vstep = r < 20 ? 10240 : 0;
    bfv8 Vp1 = F[0];
#define TILE(C0, C1, N0, N1, ABID, VLOAD, KNEXT) do { \
        bfv8 V0, V1; VLOAD; SBAR(); \
        EX(C0, 0); EX(C0, 1); SBAR(); SMF(o1, Vp1, N1, 8); SBAR(); EX(C0, 2); EX(C0, 3); EX(C0, 4); SBAR(); \
        f32x32 t = MF2B(Kr, Qr, negm, 1, ABID); SBAR(); \
        EX(C0, 5); EX(C0, 6); EX(C0, 7); EX(C0, 8); EX(C0, 9); EX(C0, 10); EX(C0, 11); EX(C0, 12); SBAR(); \
        SMF(o0, V0, C0, 0); SBAR(); EX(C0, 13); EX(C0, 14); EX(C0, 15); SBAR(); EX(C1, 0); EX(C1, 1); SBAR(); \
        SMF(o0, V1, C0, 8); SBAR(); EX(C1, 2); EX(C1, 3); EX(C1, 4); SBAR(); \
        N0 = MFMA32(Kn, Q0, LO16(t)); SBAR(); EX(C1, 5); EX(C1, 6); EX(C1, 7); EX(C1, 8); SBAR(); \
        N1 = MFMA32(Kn, Q1, HI16(t)); SBAR(); KNEXT; EX(C1, 9); EX(C1, 10); EX(C1, 11); EX(C1, 12); SBAR(); \
        SMF(o1, V0, C1, 0); SBAR(); EX(C1, 13); EX(C1, 14); EX(C1, 15); SBAR(); Vp1 = V1; } while (0)
    asm volatile("s_waitcnt vmcnt(16) lgkmcnt(0)\n\ts_barrier" ::: "memory");
    TILE(a0, a1, b0, b1, 1, (V0 = F[0], V1 = F[1]), (Kn = ldk(kp), Kr = *(LAS const s16x4*)rp));
    TILE(b0, b1, a0, a1, 0, (V0 = F[2], V1 = F[3]), (Kn = ldk(kp + 256)));
#pragma unroll 1
    for (int it = 1; it < 16; ++it) {
        if (it == 2) asm volatile("s_waitcnt vmcnt(0)\n\ts_barrier" ::: "memory");
        TILE(a0, a1, b0, b1, 1, (V0 = *(LAS const bfv8*)vp, V1 = *(LAS const bfv8*)(vp + 640)), (kp += 10240, rp += 10240, Kn = ldk(kp), Kr = *(LAS const s16x4*)rp));
        TILE(b0, b1, a0, a1, 0, (V0 = *(LAS const bfv8*)(vp + 1280), V1 = *(LAS const bfv8*)(vp + 1920)), (Kn = ldk(kp + 256)));
        vp += vstep;
    }
    SMF(o1, Vp1, b1, 8);
    __syncthreads();
    {
        LAS float* ex = (LAS float*)lds + wid * (13 * 64) + lane;
        const f32x16& snd = kh ? o0 : o1;
#pragma unroll
        for (int i = 0; i < 12; ++i) ex[i * 64] = snd[i];
        ex[12 * 64] = kh ? m0 : m1;
    }
    __syncthreads();
    LAS const float* rx = (LAS const float*)lds + (wid ^ 4) * (13 * 64) + lane;
    f32x16 o = kh ? o1 : o0; const float mo = kh ? m1 : m0, mr = rx[12 * 64], mm = fmaxf(mo, mr);
    const float fo = __builtin_amdgcn_exp2f(mo - mm), fr = __builtin_amdgcn_exp2f(mr - mm);
    float chk = 0.f;
#pragma unroll
    for (int i = 0; i < 12; ++i) { const float v = rx[i * 64]; chk += fabsf(v) + fabsf(o[i]); o[i] = o[i] * fo + v * fr; }
    const float lo = hival(kh ? o1[8] : o0[8]), lr = hival(rx[8 * 64]);
    const bool bad = !(chk < 3.0e38f) || !(lo < 1.0e18f) || !(lr < 1.0e18f);
    char* odst = ws + WS_O + O_BR + (size_t)(qb * 4 + qp) * 81920 + head * 2560 + kh * 256 + r * 8;
    if (!__any(bad)) { store_o(odst, o, gate_b / hival(o[8]), h2); return; }
    {
        float q[20], acc[20]; float m = -3.0e38f, l = 0.f;
        const unsigned short* qs = (const unsigned short*)(qg + kh * 256 + r * 8);
#pragma unroll
        for (int d = 0; d < 20; ++d) { q[d] = bf2f(qs[(d >> 2) * 256 + (d & 3)]); acc[d] = 0.f; }
#pragma unroll 1
        for (int key = 0; key < S; ++key) {
            const unsigned short* kk = (const unsigned short*)(kvg + (key >> 6) * 5120 + (key & 63) * 8);
            const unsigned short* vv = (const unsigned short*)(kvg + (key >> 6) * 5120 + 2560 + ((key & 63) >> 3) * 320) + (key & 7);
            float s = 0.f;
#pragma unroll
            for (int d = 0; d < 20; ++d) s += q[d] * bf2f(kk[(d >> 2) * 256 + (d & 3)]);
            const float mn = fmaxf(m, s), f = __builtin_amdgcn_exp2f(m - mn), p = __builtin_amdgcn_exp2f(s - mn);
            l = l * f + p; m = mn;
#pragma unroll
            for (int d = 0; d < 20; ++d) acc[d] = acc[d] * f + p * bf2f(vv[d * 8]);
        }
        const float sc = gate_b / l;
        if (h2 == 0) {
#pragma unroll
            for (int g = 0; g < 5; ++g) *(u32x2*)(odst + g * 512) = u32x2{cvtpk(acc[4 * g] * sc, acc[4 * g + 1] * sc), cvtpk(acc[4 * g + 2] * sc, acc[4 * g + 3] * sc)};
        }
    }
}

__global__ __launch_bounds__(512, 2) void k3_out(const char* __restrict__ ws, char* __restrict__ out) {
    extern __shared__ __attribute__((aligned(16))) char lds[];
    const int b = blockIdx.x, tid = threadIdx.x, lane = tid & 63, wid = __builtin_amdgcn_readfirstlane(tid >> 6);
    const int xc = b & 7, j = b >> 3, pair = (xc >> 1) * 8 + (j >> 2), cb = (xc & 1) * 4 + (j & 3);
    const int rt = wid & 3, br = wid >> 2, r16 = lane & 15, gq = lane >> 4;
    const char* op = ws + WS_O + br * O_BR + (size_t)pair * 81920 + gq * 1024 + (rt * 16 + r16) * 8;
    const char* wp = ws + WS_WT + br * WT_BR + (size_t)cb * 5 * 20 * 1024 + lane * 16;
    f32x4 acc[5] = {};
#pragma unroll 4
    for (int ks = 0; ks < 20; ++ks) {
        const bfv8 of = gldk(op + ks * 4096);
#pragma unroll
        for (int n = 0; n < 5; ++n) acc[n] = __builtin_amdgcn_mfma_f32_16x16x32_bf16(*(const bfv8*)(wp + (n * 20 + ks) * 1024), of, acc[n], 0, 0, 0);
    }
    LAS f32x4* ex = (LAS f32x4*)lds + rt * 320 + lane;
    if (br) { for (int n = 0; n < 5; ++n) ex[n * 64] = acc[n]; }
    __syncthreads();
    if (!br) {
        char* dst = out + (size_t)(pair * 64 + rt * 16 + r16) * 1280 + cb * 160 + gq * 8;
#pragma unroll
        for (int n = 0; n < 5; ++n) { const f32x4 v = acc[n] + ex[n * 64]; *(u32x2*)(dst + n * 32) = u32x2{cvtpk(v[0], v[1]), cvtpk(v[2], v[3])}; }
    }
}

extern "C" void kernel_launch(void* const* d_in, const int* in_sizes, int n_in, void* d_out, int out_size, void* d_ws, size_t ws_size, hipStream_t stream) {
    (void)hipFuncSetAttribute((const void*)k1_qkv, hipFuncAttributeMaxDynamicSharedMemorySize, K1_LDS);
    (void)hipFuncSetAttribute((const void*)k2_attn, hipFuncAttributeMaxDynamicSharedMemorySize, K2_LDS);
    k1_qkv<<<256, 512, K1_LDS, stream>>>((const char*)d_in[0], (const char*)d_in[1], (const char*)d_in[4], (const char*)d_in[2], (const char*)d_in[5], (const char*)d_in[6], (char*)d_ws);
    k2_attn<<<256, 512, K2_LDS, stream>>>((char*)d_ws);
    k3_out<<<256, 512, 20480, stream>>>((const char*)d_ws, (char*)d_out);
}
```

```cpp
#include <hip/hip_runtime.h>
#include <hip/hip_bf16.h>

typedef float f32x32 __attribute__((ext_vector_type(32)));
typedef float f32x16 __attribute__((ext_vector_type(16)));
typedef float f32x4 __attribute__((ext_vector_type(4)));
typedef short s16x4 __attribute__((ext_vector_type(4)));
typedef unsigned u32x2 __attribute__((ext_vector_type(2)));
typedef unsigned u32x4 __attribute__((ext_vector_type(4)));
typedef unsigned u32x8 __attribute__((ext_vector_type(8)));
typedef unsigned u32x16 __attribute__((ext_vector_type(16)));
typedef __bf16 bfv8 __attribute__((ext_vector_type(8)));
typedef __bf16 bfv16 __attribute__((ext_vector_type(16)));
typedef unsigned long long u64;
#define LAS __attribute__((address_space(3)))
#define GAS __attribute__((address_space(1)))
#define SBAR() __builtin_amdgcn_sched_barrier(0)
#define RLX_AGENT __ATOMIC_RELAXED, __HIP_MEMORY_SCOPE_AGENT

constexpr float QSCALE = 0.22360679774997896f * 1.4426950408889634f;
constexpr unsigned PAIRB = 81920, SECB = 32 * PAIRB;
constexpr unsigned WS_T = 0;
constexpr unsigned WS_O = 6 * SECB;
constexpr unsigned WS_WT = WS_O + 2 * SECB;
constexpr unsigned WTB = 8 * 20 * 5 * 1024;
constexpr unsigned WS_G = WS_WT + 2 * WTB;
constexpr unsigned WS_F1 = WS_G + 8192, WS_F3 = WS_F1 + 960 * 128, WS_F4 = WS_F3 + 2048 * 128, WS_F2 = WS_F4 + 32768;
constexpr unsigned WS_N = WS_F2 + 32768;
constexpr unsigned WS_END = WS_N + 256;
constexpr unsigned L_S0[2] = {67584, 88064}, L_S1[2] = {10240, 30720}, L_WF1[2] = {108544, 134144}, L_WF2[2] = {0, 51200};
constexpr unsigned L_Q = 0, L_PROG = 65536, LDS_BYTES = 163840;
constexpr int SPIN_MAX = 1 << 16;

#define STAMP(i)
#define ESTAMP(i)
#define STAMPI(i)
#define STAMP_OPS
#define STAMP_DECL()
#define STAMP_INIT()
#define STAMP_DUMP()

typedef float f32x2 __attribute__((ext_vector_type(2)));
typedef __bf16 bfv2 __attribute__((ext_vector_type(2)));
#define STAMP2(i)
__device__ __forceinline__ unsigned cvtpk(float lo, float hi) { return __builtin_bit_cast(unsigned, __builtin_convertvector((f32x2{lo, hi}), bfv2)); }
template <class T> __device__ __forceinline__ T lds_ld(unsigned a) { return *(LAS const T*)(size_t)a; }
template <class T> __device__ __forceinline__ void lds_st(unsigned a, T v) { *(LAS T*)(size_t)a = v; }
__device__ __forceinline__ s16x4 ldtr(unsigned a) { return __builtin_amdgcn_ds_read_tr16_b64_v4i16((LAS s16x4*)(size_t)a); }
__device__ __forceinline__ bfv8 cat(s16x4 a, s16x4 b) { return __builtin_bit_cast(bfv8, __builtin_shufflevector(a, b, 0, 1, 2, 3, 4, 5, 6, 7)); }
__device__ __forceinline__ bfv8 cat2(u32x2 a, u32x2 b) { return __builtin_bit_cast(bfv8, u32x4{a[0], a[1], b[0], b[1]}); }
__device__ __forceinline__ float bf2f(unsigned short v) { return __uint_as_float((unsigned)v << 16); }
__device__ __forceinline__ void dma(unsigned voff, const char* sbase, unsigned ldst) {
    asm volatile("s_mov_b32 m0, %2\n\ts_nop 0\n\tglobal_load_lds_dwordx4 %0, %1" :: "v"(voff), "s"(sbase), "s"(ldst) : "memory"); }
__device__ __forceinline__ void dma_dev(unsigned voff, const char* sbase, unsigned ldst) {
    asm volatile("s_mov_b32 m0, %2\n\ts_nop 0\n\tglobal_load_lds_dwordx4 %0, %1 sc1" :: "v"(voff), "s"(sbase), "s"(ldst) : "memory"); }
#define HLDP(W, dst, vo, sb, imm) asm volatile("global_load_" W " %0, %1, %2 offset:" #imm : "=v"(dst) : "v"(vo), "s"(sb) : "memory")
#define HLD(W, dst, vo, sb, imm) asm volatile("global_load_" W " %0, %1, %2 offset:" #imm " sc1" : "=v"(dst) : "v"(vo), "s"(sb) : "memory")
#define WAIT_BAR(n) asm volatile("s_waitcnt vmcnt(" #n ") lgkmcnt(0)\n\ts_barrier" ::: "memory")
#define VM_BAR(n) asm volatile("s_waitcnt vmcnt(" #n ")\n\ts_barrier" ::: "memory")
#define VMB(n) asm volatile("s_waitcnt vmcnt(%0)\n\ts_barrier" :: "i"(n) : "memory")
#define MFMA32(a, b, c) __builtin_amdgcn_mfma_f32_32x32x16_bf16(a, b, c, 0, 0, 0)
#define MFMA16(a, b, c) __builtin_amdgcn_mfma_f32_16x16x32_bf16(a, b, c, 0, 0, 0)
#define MF2B(a, b, c, cbsz, abid) __builtin_amdgcn_mfma_f32_32x32x4bf16_1k(a, b, c, cbsz, abid, 0)
#define Q8(s, o) __builtin_bit_cast(bfv16, __builtin_shufflevector(s, s, o, o + 1, o + 2, o + 3, o + 4, o + 5, o + 6, o + 7))
#define LO16(s) __builtin_shufflevector(s, s, 0, 1, 2, 3, 4, 5, 6, 7, 8, 9, 10, 11, 12, 13, 14, 15)
#define HI16(s) __builtin_shufflevector(s, s, 16, 17, 18, 19, 20, 21, 22, 23, 24, 25, 26, 27, 28, 29, 30, 31)
#define SMF(o, v, s, off) o = __builtin_amdgcn_smfmac_f32_32x32x32_bf16(v, Q8(s, off), o, idx, 0, 0)
#define LD64(off) __builtin_bit_cast(u32x2, __builtin_amdgcn_raw_buffer_load_b64(rs, off, 0, 16))
#define LD128(off) __builtin_bit_cast(u32x4, __builtin_amdgcn_raw_buffer_load_b128(rs, off, 0, 16))
#define LD32(off) __builtin_amdgcn_raw_buffer_load_b32(rs, off, 0, 16)
#define ST64(v, off) __builtin_amdgcn_raw_buffer_store_b64(v, rs, off, 0, 16)
#define ST128(v, off) __builtin_amdgcn_raw_buffer_store_b128(v, rs, off, 0, 16)
__device__ __forceinline__ float swapmax(float m) { auto rr = __builtin_amdgcn_permlane32_swap(__float_as_uint(m), __float_as_uint(m), false, false); return fmaxf(__uint_as_float(rr[0]), __uint_as_float(rr[1])); }
__device__ __forceinline__ float hival(float m) { auto rr = __builtin_amdgcn_permlane32_swap(__float_as_uint(m), __float_as_uint(m), false, false); return __uint_as_float(rr[1]); }
__device__ __forceinline__ float max16(const f32x16& p) { float a = fmaxf(fmaxf(p[0], p[1]), p[2]);
#pragma unroll
    for (int i = 3; i < 15; i += 2) a = fmaxf(fmaxf(a, p[i]), p[i + 1]);
    return fmaxf(a, p[15]); }
#define ATT_ALL \
    "s_branch .Latt_a\n" \
    ".Latt_spin1:\n" \
    "ds_read_b32 v95, v94\n" \
    "s_sub_u32 s50, s50, 1\n" \
    "s_cbranch_scc1 .Latt_go1\n" \
    "s_waitcnt lgkmcnt(0)\n" \
    "v_cmp_gt_u32 vcc, s46, v95\n" \
    "s_nop 0\n" \
    "s_cbranch_vccnz .Latt_spin1\n" \
    "s_branch .Latt_go1\n" \
    ".Latt_spin4:\n" \
    "ds_read_b32 v95, v94\n" \
    "s_sub_u32 s50, s50, 1\n" \
    "s_cbranch_scc1 .Latt_go4\n" \
    "s_waitcnt lgkmcnt(0)\n" \
    "v_cmp_gt_u32 vcc, s46, v95\n" \
    "s_nop 0\n" \
    "s_cbranch_vccnz .Latt_spin4\n" \
    "s_branch .Latt_go4\n" \
    ".Latt_spin9:\n" \
    "ds_read_b32 v95, v94\n" \
    "s_sub_u32 s50, s50, 1\n" \
    "s_cbranch_scc1 .Latt_go9\n" \
    "s_waitcnt lgkmcnt(0)\n" \
    "v_cmp_gt_u32 vcc, s46, v95\n" \
    "s_nop 0\n" \
    "s_cbranch_vccnz .Latt_spin9\n" \
    "s_branch .Latt_go9\n" \
    ".Latt_spin12:\n" \
    "ds_read_b32 v95, v94\n" \
    "s_sub_u32 s50, s50, 1\n" \
    "s_cbranch_scc1 .Latt_go12\n" \
    "s_waitcnt lgkmcnt(0)\n" \
    "v_cmp_gt_u32 vcc, s46, v95\n" \
    "s_nop 0\n" \
    "s_cbranch_vccnz .Latt_spin12\n" \
    "s_branch .Latt_go12\n" \
    ".Latt_spin17:\n" \
    "ds_read_b32 v95, v94\n" \
    "s_sub_u32 s50, s50, 1\n" \
    "s_cbranch_scc1 .Latt_go17\n" \
    "s_waitcnt lgkmcnt(0)\n" \
    "v_cmp_gt_u32 vcc, s46, v95\n" \
    "s_nop 0\n" \
    "s_cbranch_vccnz .Latt_spin17\n" \
    "s_branch .Latt_go17\n" \
    ".Latt_spin20:\n" \
    "ds_read_b32 v95, v94\n" \
    "s_sub_u32 s50, s50, 1\n" \
    "s_cbranch_scc1 .Latt_go20\n" \
    "s_waitcnt lgkmcnt(0)\n" \
    "v_cmp_gt_u32 vcc, s46, v95\n" \
    "s_nop 0\n" \
    "s_cbranch_vccnz .Latt_spin20\n" \
    "s_branch .Latt_go20\n" \
    ".Latt_spin25:\n" \
    "ds_read_b32 v95, v94\n" \
    "s_sub_u32 s50, s50, 1\n" \
    "s_cbranch_scc1 .Latt_go25\n" \
    "s_waitcnt lgkmcnt(0)\n" \
    "v_cmp_gt_u32 vcc, s46, v95\n" \
    "s_nop 0\n" \
    "s_cbranch_vccnz .Latt_spin25\n" \
    "s_branch .Latt_go25\n" \
    ".Latt_spin30:\n" \
    "ds_read_b32 v95, v94\n" \
    "s_sub_u32 s50, s50, 1\n" \
    "s_cbranch_scc1 .Latt_go30\n" \
    "s_waitcnt lgkmcnt(0)\n" \
    "v_cmp_gt_u32 vcc, s46, v95\n" \
    "s_nop 0\n" \
    "s_cbranch_vccnz .Latt_spin30\n" \
    "s_branch .Latt_go30\n" \
    ".Latt_fspin11:\n" \
    "global_load_dwordx2 v[34:35], v5, s[40:41] sc1\n" \
    "s_sub_u32 s50, s50, 1\n" \
    "s_waitcnt vmcnt(0)\n" \
    "s_cbranch_scc1 .Latt_fgo11\n" \
    "v_cmp_ne_u64 vcc, %[tag], v[34:35]\n" \
    "s_sleep 2\n" \
    "s_cbranch_vccnz .Latt_fspin11\n" \
    "s_branch .Latt_fgo11\n" \
    ".Latt_fspin27:\n" \
    "global_load_dwordx2 v[34:35], v5, s[40:41] sc1\n" \
    "s_sub_u32 s50, s50, 1\n" \
    "s_waitcnt vmcnt(0)\n" \
    "s_cbranch_scc1 .Latt_fgo27\n" \
    "v_cmp_ne_u64 vcc, %[tag], v[34:35]\n" \
    "s_sleep 2\n" \
    "s_cbranch_vccnz .Latt_fspin27\n" \
    "s_branch .Latt_fgo27\n" \
    ".Latt_ret:\n" \
    "v_lshlrev_b32 v35, 3, v92\n" \
    "s_mov_b32 s50, 0x10000\n" \
    "1:\n" \
    "s_add_u32 s58, s40, 23035904\n" \
    "s_addc_u32 s59, s41, 0\n" \
    "global_load_dwordx2 v[36:37], v35, s[58:59] sc1\n" \
    "s_add_u32 s58, s58, 8192\n" \
    "s_addc_u32 s59, s59, 0\n" \
    "global_load_dwordx2 v[38:39], v35, s[58:59] sc1\n" \
    "s_add_u32 s58, s58, 8192\n" \
    "s_addc_u32 s59, s59, 0\n" \
    "global_load_dwordx2 v[40:41], v35, s[58:59] sc1\n" \
    "s_add_u32 s58, s58, 8192\n" \
    "s_addc_u32 s59, s59, 0\n" \
    "global_load_dwordx2 v[42:43], v35, s[58:59] sc1\n" \
    "s_add_u32 s58, s58, 8192\n" \
    "s_addc_u32 s59, s59, 0\n" \
    "s_sub_u32 s50, s50, 1\n" \
    "s_waitcnt vmcnt(0)\n" \
    "s_cbranch_scc1 2f\n" \
    "v_cmp_ne_u64 vcc, %[tag], v[36:37]\n" \
    "s_nop 0\n" \
    "s_cbranch_vccnz 1b\n" \
    "v_cmp_ne_u64 vcc, %[tag], v[38:39]\n" \
    "s_nop 0\n" \
    "s_cbranch_vccnz 1b\n" \
    "v_cmp_ne_u64 vcc, %[tag], v[40:41]\n" \
    "s_nop 0\n" \
    "s_cbranch_vccnz 1b\n" \
    "v_cmp_ne_u64 vcc, %[tag], v[42:43]\n" \
    "s_nop 0\n" \
    "s_cbranch_vccnz 1b\n" \
    "2:\n" \
    "s_mov_b64 exec, 1\n" \
    "v_mov_b64 v[34:35], %[tag]\n" \
    "global_store_dwordx2 v92, v[34:35], s[58:59] sc1\n" \
    "s_mov_b64 exec, -1\n" \
    "s_branch .Latt_b\n" \
    ".p2align 12\n" \
    ".Latt_a:\n" \
    ".Lis0: s_cbranch_execz .Lis_ret\n" \
    "s_setprio 0\n" \
    "v_exp_f32 v96, v96\n" \
    "s_setprio 1\n" \
    "v_exp_f32 v97, v97\n" \
    "v_exp_f32 v98, v98\n" \
    "v_exp_f32 v99, v99\n" \
    "v_exp_f32 v100, v100\n" \
    "v_exp_f32 v101, v101\n" \
    "v_exp_f32 v102, v102\n" \
    "v_exp_f32 v103, v103\n" \
    "v_exp_f32 v104, v104\n" \
    "v_exp_f32 v105, v105\n" \
    "v_exp_f32 v106, v106\n" \
    "v_exp_f32 v107, v107\n" \
    "v_exp_f32 v108, v108\n" \
    "v_exp_f32 v109, v109\n" \
    "v_exp_f32 v110, v110\n" \
    "v_exp_f32 v111, v111\n" \
    "v_exp_f32 v112, v112\n" \
    "v_exp_f32 v113, v113\n" \
    "v_exp_f32 v114, v114\n" \
    "v_exp_f32 v115, v115\n" \
    "v_exp_f32 v116, v116\n" \
    "v_exp_f32 v117, v117\n" \
    "v_exp_f32 v118, v118\n" \
    "v_exp_f32 v119, v119\n" \
    "v_exp_f32 v120, v120\n" \
    "v_exp_f32 v121, v121\n" \
    "v_exp_f32 v122, v122\n" \
    "v_exp_f32 v123, v123\n" \
    "v_exp_f32 v124, v124\n" \
    "v_exp_f32 v125, v125\n" \
    "v_exp_f32 v126, v126\n" \
    "v_exp_f32 v127, v127\n" \
    "s_setprio 2\n" \
    "s_waitcnt lgkmcnt(0)\n" \
    "v_mfma_f32_32x32x4_2b_bf16 v[128:159], v[234:235], v[232:233], v[160:191] cbsz:1 abid:1\n" \
    "s_add_u32 s46, s46, 1\n" \
    "v_mov_b32 v95, s46\n" \
    "ds_write_b32 v93, v95\n" \
    "s_mov_b64 exec, s[48:49]\n" \
    "ds_read_b128 v[248:251], v86 offset:1280\n" \
    "ds_read_b128 v[252:255], v86 offset:1920\n" \
    "s_mov_b64 exec, -1\n" \
    "s_nop 1\n" \
    "v_smfmac_f32_32x32x32_bf16 v[192:207], v[240:243], v[96:103], v84\n" \
    "s_nop 2\n" \
    "v_smfmac_f32_32x32x32_bf16 v[192:207], v[244:247], v[104:111], v84\n" \
    "s_nop 2\n" \
    "v_smfmac_f32_32x32x32_bf16 v[208:223], v[240:243], v[112:119], v84\n" \
    "v_mfma_f32_32x32x16_bf16 v[128:143], v[236:239], v[224:227], v[128:143]\n" \
    "s_nop 3\n" \
    "v_mfma_f32_32x32x16_bf16 v[144:159], v[236:239], v[228:231], v[144:159]\n" \
    "ds_read_b64 v[236:237], v85 offset:5120\n" \
    "ds_read_b64 v[238:239], v85 offset:5632\n" \
    "ds_read_b64 v[234:235], v85 offset:6144\n" \
    "s_nop 1\n" \
    "v_smfmac_f32_32x32x32_bf16 v[208:223], v[244:247], v[120:127], v84\n" \
    "s_setprio 0\n" \
    "v_exp_f32 v128, v128\n" \
    "s_setprio 1\n" \
    "v_exp_f32 v129, v129\n" \
    "v_exp_f32 v130, v130\n" \
    "v_exp_f32 v131, v131\n" \
    "v_exp_f32 v132, v132\n" \
    "v_exp_f32 v133, v133\n" \
    "v_exp_f32 v134, v134\n" \
    "v_exp_f32 v135, v135\n" \
    "v_exp_f32 v136, v136\n" \
    "v_exp_f32 v137, v137\n" \
    "v_exp_f32 v138, v138\n" \
    "v_exp_f32 v139, v139\n" \
    "v_exp_f32 v140, v140\n" \
    "v_exp_f32 v141, v141\n" \
    "v_exp_f32 v142, v142\n" \
    "v_exp_f32 v143, v143\n" \
    "v_exp_f32 v144, v144\n" \
    "v_exp_f32 v145, v145\n" \
    "v_exp_f32 v146, v146\n" \
    "v_exp_f32 v147, v147\n" \
    "v_exp_f32 v148, v148\n" \
    "v_exp_f32 v149, v149\n" \
    "v_exp_f32 v150, v150\n" \
    "v_exp_f32 v151, v151\n" \
    "v_exp_f32 v152, v152\n" \
    "v_exp_f32 v153, v153\n" \
    "v_exp_f32 v154, v154\n" \
    "v_exp_f32 v155, v155\n" \
    "v_exp_f32 v156, v156\n" \
    "v_exp_f32 v157, v157\n" \
    "v_exp_f32 v158, v158\n" \
    "v_exp_f32 v159, v159\n" \
    "s_setprio 2\n" \
    "s_waitcnt lgkmcnt(0)\n" \
    "v_mfma_f32_32x32x4_2b_bf16 v[96:127], v[234:235], v[232:233], v[160:191] cbsz:1 abid:0\n" \
    "ds_read_b32 v95, v94\n" \
    "s_mov_b64 exec, s[48:49]\n" \
    "ds_read_b128 v[240:243], v86 offset:5120\n" \
    "ds_read_b128 v[244:247], v86 offset:5760\n" \
    "s_mov_b64 exec, -1\n" \
    "s_nop 3\n" \
    "v_smfmac_f32_32x32x32_bf16 v[192:207], v[248:251], v[128:135], v84\n" \
    "s_waitcnt lgkmcnt(2)\n" \
    "v_cmp_gt_u32 vcc, s46, v95\n" \
    "s_movk_i32 s50, 0x4000\n" \
    "s_cbranch_vccnz .Latt_spin1\n" \
    ".Latt_go1:\n" \
    "v_smfmac_f32_32x32x32_bf16 v[192:207], v[252:255], v[136:143], v84\n" \
    "s_add_u32 m0, s44, 0\n" \
    "s_nop 0\n" \
    "global_load_lds_dwordx4 v87, s[42:43] sc1\n" \
    "v_smfmac_f32_32x32x32_bf16 v[208:223], v[248:251], v[144:151], v84\n" \
    "s_add_u32 m0, s44, 1024\n" \
    "s_nop 0\n" \
    "global_load_lds_dwordx4 v88, s[42:43] sc1\n" \
    "v_mfma_f32_32x32x16_bf16 v[96:111], v[236:239], v[224:227], v[96:111]\n" \
    "s_add_u32 m0, s44, 2048\n" \
    "s_nop 0\n" \
    "global_load_lds_dwordx4 v89, s[42:43] sc1\n" \
    "s_nop 0\n" \
    "v_mfma_f32_32x32x16_bf16 v[112:127], v[236:239], v[228:231], v[112:127]\n" \
    "ds_read_b64 v[236:237], v85 offset:5376\n" \
    "ds_read_b64 v[238:239], v85 offset:5888\n" \
    "s_add_u32 m0, s44, 3072\n" \
    "s_nop 0\n" \
    "global_load_lds_dwordx4 v90, s[42:43] sc1\n" \
    "s_add_u32 m0, s44, 4096\n" \
    "s_nop 0\n" \
    "global_load_lds_dwordx4 v91, s[42:43] sc1\n" \
    "s_add_u32 s42, s42, 327680\n" \
    "s_addc_u32 s43, s43, 0\n" \
    "v_smfmac_f32_32x32x32_bf16 v[208:223], v[252:255], v[152:159], v84\n" \
    "s_setprio 0\n" \
    "v_exp_f32 v96, v96\n" \
    "s_setprio 1\n" \
    "v_exp_f32 v97, v97\n" \
    "v_exp_f32 v98, v98\n" \
    "v_exp_f32 v99, v99\n" \
    "v_exp_f32 v100, v100\n" \
    "v_exp_f32 v101, v101\n" \
    "v_exp_f32 v102, v102\n" \
    "v_exp_f32 v103, v103\n" \
    "v_exp_f32 v104, v104\n" \
    "v_exp_f32 v105, v105\n" \
    "v_exp_f32 v106, v106\n" \
    "v_exp_f32 v107, v107\n" \
    "v_exp_f32 v108, v108\n" \
    "v_exp_f32 v109, v109\n" \
    "v_exp_f32 v110, v110\n" \
    "v_exp_f32 v111, v111\n" \
    "v_exp_f32 v112, v112\n" \
    "v_exp_f32 v113, v113\n" \
    "v_exp_f32 v114, v114\n" \
    "v_exp_f32 v115, v115\n" \
    "v_exp_f32 v116, v116\n" \
    "v_exp_f32 v117, v117\n" \
    "v_exp_f32 v118, v118\n" \
    "v_exp_f32 v119, v119\n" \
    "v_exp_f32 v120, v120\n" \
    "v_exp_f32 v121, v121\n" \
    "v_exp_f32 v122, v122\n" \
    "v_exp_f32 v123, v123\n" \
    "v_exp_f32 v124, v124\n" \
    "v_exp_f32 v125, v125\n" \
    "v_exp_f32 v126, v126\n" \
    "v_exp_f32 v127, v127\n" \
    "s_setprio 2\n" \
    "s_waitcnt lgkmcnt(0)\n" \
    "v_mfma_f32_32x32x4_2b_bf16 v[128:159], v[234:235], v[232:233], v[160:191] cbsz:1 abid:1\n" \
    "s_mov_b64 exec, s[48:49]\n" \
    "ds_read_b128 v[248:251], v86 offset:6400\n" \
    "ds_read_b128 v[252:255], v86 offset:7040\n" \
    "s_mov_b64 exec, -1\n" \
    "s_nop 4\n" \
    "v_smfmac_f32_32x32x32_bf16 v[192:207], v[240:243], v[96:103], v84\n" \
    "s_lshr_b32 s66, %[b], 5\n" \
    "s_lshl_b32 s66, s66, 5\n" \
    "s_add_u32 s66, s66, %[head]\n" \
    "v_smfmac_f32_32x32x32_bf16 v[192:207], v[244:247], v[104:111], v84\n" \
    "s_lshl_b32 s66, s66, 1\n" \
    "s_and_b32 s67, %[wid], 1\n" \
    "s_add_u32 s66, s66, s67\n" \
    "v_smfmac_f32_32x32x32_bf16 v[208:223], v[240:243], v[112:119], v84\n" \
    "v_mfma_f32_32x32x16_bf16 v[128:143], v[236:239], v[224:227], v[128:143]\n" \
    "s_mul_i32 s66, s66, 5120\n" \
    "s_mov_b64 s[56:57], %[x]\n" \
    "s_add_u32 s56, s56, s66\n" \
    "s_addc_u32 s57, s57, 0\n" \
    "v_mfma_f32_32x32x16_bf16 v[144:159], v[236:239], v[228:231], v[144:159]\n" \
    "ds_read_b64 v[236:237], v85 offset:10240\n" \
    "ds_read_b64 v[238:239], v85 offset:10752\n" \
    "ds_read_b64 v[234:235], v85 offset:11264\n" \
    "s_nop 1\n" \
    "v_smfmac_f32_32x32x32_bf16 v[208:223], v[244:247], v[120:127], v84\n" \
    "s_setprio 0\n" \
    "v_exp_f32 v128, v128\n" \
    "s_setprio 1\n" \
    "v_exp_f32 v129, v129\n" \
    "v_exp_f32 v130, v130\n" \
    "v_exp_f32 v131, v131\n" \
    "v_exp_f32 v132, v132\n" \
    "v_exp_f32 v133, v133\n" \
    "v_exp_f32 v134, v134\n" \
    "v_exp_f32 v135, v135\n" \
    "v_exp_f32 v136, v136\n" \
    "v_exp_f32 v137, v137\n" \
    "v_exp_f32 v138, v138\n" \
    "v_exp_f32 v139, v139\n" \
    "v_exp_f32 v140, v140\n" \
    "v_exp_f32 v141, v141\n" \
    "v_exp_f32 v142, v142\n" \
    "v_exp_f32 v143, v143\n" \
    "v_exp_f32 v144, v144\n" \
    "v_exp_f32 v145, v145\n" \
    "v_exp_f32 v146, v146\n" \
    "v_exp_f32 v147, v147\n" \
    "v_exp_f32 v148, v148\n" \
    "v_exp_f32 v149, v149\n" \
    "v_exp_f32 v150, v150\n" \
    "v_exp_f32 v151, v151\n" \
    "v_exp_f32 v152, v152\n" \
    "v_exp_f32 v153, v153\n" \
    "v_exp_f32 v154, v154\n" \
    "v_exp_f32 v155, v155\n" \
    "v_exp_f32 v156, v156\n" \
    "v_exp_f32 v157, v157\n" \
    "v_exp_f32 v158, v158\n" \
    "v_exp_f32 v159, v159\n" \
    "s_setprio 2\n" \
    "s_waitcnt lgkmcnt(0)\n" \
    "v_mfma_f32_32x32x4_2b_bf16 v[96:127], v[234:235], v[232:233], v[160:191] cbsz:1 abid:0\n" \
    "s_add_u32 s46, s46, 1\n" \
    "s_waitcnt vmcnt(0)\n" \
    "v_mov_b32 v95, s46\n" \
    "ds_write_b32 v93, v95\n" \
    "s_lshl_b32 s51, %[wid], 2\n" \
    "s_or_b32 s51, s51, 0xffffffef\n" \
    "v_and_b32 v94, s51, v94\n" \
    ".Lis1: s_cbranch_execz .Lis_ret\n" \
    "s_mov_b64 exec, s[48:49]\n" \
    "ds_read_b128 v[240:243], v86 offset:10240\n" \
    "ds_read_b128 v[244:247], v86 offset:10880\n" \
    "s_mov_b64 exec, -1\n" \
    "v_smfmac_f32_32x32x32_bf16 v[192:207], v[248:251], v[128:135], v84\n" \
    "s_lshl_b32 s66, %[b], 7\n" \
    "s_add_u32 s66, s66, 23035904\n" \
    "s_add_u32 s54, s40, s66\n" \
    "s_addc_u32 s55, s41, 0\n" \
    "v_smfmac_f32_32x32x32_bf16 v[192:207], v[252:255], v[136:143], v84\n" \
    "s_cmp_eq_u32 %[wid], 4\n" \
    "s_cselect_b64 s[52:53], 1, 0\n" \
    "s_nop 0\n" \
    "v_smfmac_f32_32x32x32_bf16 v[208:223], v[248:251], v[144:151], v84\n" \
    "v_mfma_f32_32x32x16_bf16 v[96:111], v[236:239], v[224:227], v[96:111]\n" \
    "s_nop 3\n" \
    "v_mfma_f32_32x32x16_bf16 v[112:127], v[236:239], v[228:231], v[112:127]\n" \
    "ds_read_b64 v[236:237], v85 offset:10496\n" \
    "ds_read_b64 v[238:239], v85 offset:11008\n" \
    "s_nop 2\n" \
    "v_smfmac_f32_32x32x32_bf16 v[208:223], v[252:255], v[152:159], v84\n" \
    "s_setprio 0\n" \
    "v_exp_f32 v96, v96\n" \
    "s_setprio 1\n" \
    "v_exp_f32 v97, v97\n" \
    "v_exp_f32 v98, v98\n" \
    "v_exp_f32 v99, v99\n" \
    "v_exp_f32 v100, v100\n" \
    "v_exp_f32 v101, v101\n" \
    "v_exp_f32 v102, v102\n" \
    "v_exp_f32 v103, v103\n" \
    "v_exp_f32 v104, v104\n" \
    "v_exp_f32 v105, v105\n" \
    "v_exp_f32 v106, v106\n" \
    "v_exp_f32 v107, v107\n" \
    "v_exp_f32 v108, v108\n" \
    "v_exp_f32 v109, v109\n" \
    "v_exp_f32 v110, v110\n" \
    "v_exp_f32 v111, v111\n" \
    "v_exp_f32 v112, v112\n" \
    "v_exp_f32 v113, v113\n" \
    "v_exp_f32 v114, v114\n" \
    "v_exp_f32 v115, v115\n" \
    "v_exp_f32 v116, v116\n" \
    "v_exp_f32 v117, v117\n" \
    "v_exp_f32 v118, v118\n" \
    "v_exp_f32 v119, v119\n" \
    "v_exp_f32 v120, v120\n" \
    "v_exp_f32 v121, v121\n" \
    "v_exp_f32 v122, v122\n" \
    "v_exp_f32 v123, v123\n" \
    "v_exp_f32 v124, v124\n" \
    "v_exp_f32 v125, v125\n" \
    "v_exp_f32 v126, v126\n" \
    "v_exp_f32 v127, v127\n" \
    "s_setprio 2\n" \
    "s_waitcnt lgkmcnt(0)\n" \
    "v_mfma_f32_32x32x4_2b_bf16 v[128:159], v[234:235], v[232:233], v[160:191] cbsz:1 abid:1\n" \
    "ds_read_b32 v95, v94\n" \
    "s_mov_b64 exec, s[48:49]\n" \
    "ds_read_b128 v[248:251], v86 offset:11520\n" \
    "ds_read_b128 v[252:255], v86 offset:12160\n" \
    "s_mov_b64 exec, -1\n" \
    "s_nop 3\n" \
    "v_smfmac_f32_32x32x32_bf16 v[192:207], v[240:243], v[96:103], v84\n" \
    "s_waitcnt lgkmcnt(2)\n" \
    "v_cmp_gt_u32 vcc, s46, v95\n" \
    "s_movk_i32 s50, 0x4000\n" \
    "s_cbranch_vccnz .Latt_spin4\n" \
    ".Latt_go4:\n" \
    "v_smfmac_f32_32x32x32_bf16 v[192:207], v[244:247], v[104:111], v84\n" \
    "s_mov_b64 exec, s[52:53]\n" \
    "v_mov_b64 v[34:35], %[tag]\n" \
    "global_store_dwordx2 v92, v[34:35], s[54:55] sc1\n" \
    "s_mov_b64 exec, -1\n" \
    "v_smfmac_f32_32x32x32_bf16 v[208:223], v[240:243], v[112:119], v84\n" \
    "v_mfma_f32_32x32x16_bf16 v[128:143], v[236:239], v[224:227], v[128:143]\n" \
    "s_lshr_b32 s51, %[wid], 1\n" \
    "s_cmp_eq_u32 s51, 2\n" \
    "s_cselect_b64 s[52:53], -1, 0\n" \
    "s_nop 0\n" \
    "v_mfma_f32_32x32x16_bf16 v[144:159], v[236:239], v[228:231], v[144:159]\n" \
    "ds_read_b64 v[236:237], v85 offset:15360\n" \
    "ds_read_b64 v[238:239], v85 offset:15872\n" \
    "ds_read_b64 v[234:235], v85 offset:16384\n" \
    "s_nop 1\n" \
    "v_smfmac_f32_32x32x32_bf16 v[208:223], v[244:247], v[120:127], v84\n" \
    "s_setprio 0\n" \
    "v_exp_f32 v128, v128\n" \
    "s_setprio 1\n" \
    "v_exp_f32 v129, v129\n" \
    "v_exp_f32 v130, v130\n" \
    "v_exp_f32 v131, v131\n" \
    "v_exp_f32 v132, v132\n" \
    "v_exp_f32 v133, v133\n" \
    "v_exp_f32 v134, v134\n" \
    "v_exp_f32 v135, v135\n" \
    "v_exp_f32 v136, v136\n" \
    "v_exp_f32 v137, v137\n" \
    "v_exp_f32 v138, v138\n" \
    "v_exp_f32 v139, v139\n" \
    "v_exp_f32 v140, v140\n" \
    "v_exp_f32 v141, v141\n" \
    "v_exp_f32 v142, v142\n" \
    "v_exp_f32 v143, v143\n" \
    "v_exp_f32 v144, v144\n" \
    "v_exp_f32 v145, v145\n" \
    "v_exp_f32 v146, v146\n" \
    "v_exp_f32 v147, v147\n" \
    "v_exp_f32 v148, v148\n" \
    "v_exp_f32 v149, v149\n" \
    "v_exp_f32 v150, v150\n" \
    "v_exp_f32 v151, v151\n" \
    "v_exp_f32 v152, v152\n" \
    "v_exp_f32 v153, v153\n" \
    "v_exp_f32 v154, v154\n" \
    "v_exp_f32 v155, v155\n" \
    "v_exp_f32 v156, v156\n" \
    "v_exp_f32 v157, v157\n" \
    "v_exp_f32 v158, v158\n" \
    "v_exp_f32 v159, v159\n" \
    "s_setprio 2\n" \
    "s_waitcnt lgkmcnt(0)\n" \
    "v_mfma_f32_32x32x4_2b_bf16 v[96:127], v[234:235], v[232:233], v[160:191] cbsz:1 abid:0\n" \
    "s_mov_b64 exec, s[48:49]\n" \
    "ds_read_b128 v[240:243], v86 offset:15360\n" \
    "ds_read_b128 v[244:247], v86 offset:16000\n" \
    "s_mov_b64 exec, -1\n" \
    "s_nop 4\n" \
    "v_smfmac_f32_32x32x32_bf16 v[192:207], v[248:251], v[128:135], v84\n" \
    "s_mov_b64 exec, s[52:53]\n" \
    "global_load_dwordx4 v[36:39], v0, s[56:57] offset:0\n" \
    "s_mov_b64 exec, -1\n" \
    "v_smfmac_f32_32x32x32_bf16 v[192:207], v[252:255], v[136:143], v84\n" \
    "s_mov_b64 exec, s[52:53]\n" \
    "global_load_dwordx4 v[40:43], v0, s[56:57] offset:256\n" \
    "s_mov_b64 exec, -1\n" \
    "v_smfmac_f32_32x32x32_bf16 v[208:223], v[248:251], v[144:151], v84\n" \
    "s_mov_b64 exec, s[52:53]\n" \
    "global_load_dwordx4 v[44:47], v0, s[56:57] offset:512\n" \
    "s_mov_b64 exec, -1\n" \
    "v_mfma_f32_32x32x16_bf16 v[96:111], v[236:239], v[224:227], v[96:111]\n" \
    "s_mov_b64 exec, s[52:53]\n" \
    "global_load_dwordx4 v[48:51], v0, s[56:57] offset:768\n" \
    "s_mov_b64 exec, -1\n" \
    "s_nop 0\n" \
    "v_mfma_f32_32x32x16_bf16 v[112:127], v[236:239], v[228:231], v[112:127]\n" \
    "ds_read_b64 v[236:237], v85 offset:15616\n" \
    "ds_read_b64 v[238:239], v85 offset:16128\n" \
    "s_mov_b64 exec, s[52:53]\n" \
    "global_load_dwordx4 v[52:55], v0, s[56:57] offset:1024\n" \
    "s_mov_b64 exec, -1\n" \
    "v_smfmac_f32_32x32x32_bf16 v[208:223], v[252:255], v[152:159], v84\n" \
    "s_setprio 0\n" \
    "v_exp_f32 v96, v96\n" \
    "s_setprio 1\n" \
    "v_exp_f32 v97, v97\n" \
    "v_exp_f32 v98, v98\n" \
    "v_exp_f32 v99, v99\n" \
    "v_exp_f32 v100, v100\n" \
    "v_exp_f32 v101, v101\n" \
    "v_exp_f32 v102, v102\n" \
    "v_exp_f32 v103, v103\n" \
    "v_exp_f32 v104, v104\n" \
    "v_exp_f32 v105, v105\n" \
    "v_exp_f32 v106, v106\n" \
    "v_exp_f32 v107, v107\n" \
    "v_exp_f32 v108, v108\n" \
    "v_exp_f32 v109, v109\n" \
    "v_exp_f32 v110, v110\n" \
    "v_exp_f32 v111, v111\n" \
    "v_exp_f32 v112, v112\n" \
    "v_exp_f32 v113, v113\n" \
    "v_exp_f32 v114, v114\n" \
    "v_exp_f32 v115, v115\n" \
    "v_exp_f32 v116, v116\n" \
    "v_exp_f32 v117, v117\n" \
    "v_exp_f32 v118, v118\n" \
    "v_exp_f32 v119, v119\n" \
    "v_exp_f32 v120, v120\n" \
    "v_exp_f32 v121, v121\n" \
    "v_exp_f32 v122, v122\n" \
    "v_exp_f32 v123, v123\n" \
    "v_exp_f32 v124, v124\n" \
    "v_exp_f32 v125, v125\n" \
    "v_exp_f32 v126, v126\n" \
    "v_exp_f32 v127, v127\n" \
    "s_setprio 2\n" \
    "s_waitcnt lgkmcnt(0)\n" \
    "v_mfma_f32_32x32x4_2b_bf16 v[128:159], v[234:235], v[232:233], v[160:191] cbsz:1 abid:1\n" \
    "v_add_u32 v85, 0xffff2000, v85\n" \
    "s_mov_b64 exec, s[48:49]\n" \
    "ds_read_b128 v[248:251], v86 offset:16640\n" \
    "ds_read_b128 v[252:255], v86 offset:17280\n" \
    "s_mov_b64 exec, -1\n" \
    "s_nop 3\n" \
    "v_smfmac_f32_32x32x32_bf16 v[192:207], v[240:243], v[96:103], v84\n" \
    "s_mov_b64 exec, s[52:53]\n" \
    "global_load_dwordx4 v[56:59], v1, %[gw] offset:0\n" \
    "s_mov_b64 exec, -1\n" \
    "v_smfmac_f32_32x32x32_bf16 v[192:207], v[244:247], v[104:111], v84\n" \
    "s_mov_b64 exec, s[52:53]\n" \
    "global_load_dwordx4 v[60:63], v1, %[gw] offset:256\n" \
    "s_mov_b64 exec, -1\n" \
    "v_smfmac_f32_32x32x32_bf16 v[208:223], v[240:243], v[112:119], v84\n" \
    "s_mov_b64 exec, s[52:53]\n" \
    "global_load_dwordx4 v[64:67], v1, %[gw] offset:512\n" \
    "s_mov_b64 exec, -1\n" \
    "v_mfma_f32_32x32x16_bf16 v[128:143], v[236:239], v[224:227], v[128:143]\n" \
    "s_mov_b64 exec, s[52:53]\n" \
    "global_load_dwordx4 v[68:71], v1, %[gw] offset:768\n" \
    "s_mov_b64 exec, -1\n" \
    "s_nop 0\n" \
    "v_mfma_f32_32x32x16_bf16 v[144:159], v[236:239], v[228:231], v[144:159]\n" \
    "ds_read_b64 v[236:237], v85 offset:0\n" \
    "ds_read_b64 v[238:239], v85 offset:512\n" \
    "ds_read_b64 v[234:235], v85 offset:1024\n" \
    "s_mov_b64 exec, s[52:53]\n" \
    "global_load_dwordx4 v[72:75], v1, %[gw] offset:1024\n" \
    "s_mov_b64 exec, -1\n" \
    "v_smfmac_f32_32x32x32_bf16 v[208:223], v[244:247], v[120:127], v84\n" \
    "s_setprio 0\n" \
    "v_exp_f32 v128, v128\n" \
    "s_setprio 1\n" \
    "v_exp_f32 v129, v129\n" \
    "v_exp_f32 v130, v130\n" \
    "v_exp_f32 v131, v131\n" \
    "v_exp_f32 v132, v132\n" \
    "v_exp_f32 v133, v133\n" \
    "v_exp_f32 v134, v134\n" \
    "v_exp_f32 v135, v135\n" \
    "v_exp_f32 v136, v136\n" \
    "v_exp_f32 v137, v137\n" \
    "v_exp_f32 v138, v138\n" \
    "v_exp_f32 v139, v139\n" \
    "v_exp_f32 v140, v140\n" \
    "v_exp_f32 v141, v141\n" \
    "v_exp_f32 v142, v142\n" \
    "v_exp_f32 v143, v143\n" \
    "v_exp_f32 v144, v144\n" \
    "v_exp_f32 v145, v145\n" \
    "v_exp_f32 v146, v146\n" \
    "v_exp_f32 v147, v147\n" \
    "v_exp_f32 v148, v148\n" \
    "v_exp_f32 v149, v149\n" \
    "v_exp_f32 v150, v150\n" \
    "v_exp_f32 v151, v151\n" \
    "v_exp_f32 v152, v152\n" \
    "v_exp_f32 v153, v153\n" \
    "v_exp_f32 v154, v154\n" \
    "v_exp_f32 v155, v155\n" \
    "v_exp_f32 v156, v156\n" \
    "v_exp_f32 v157, v157\n" \
    "v_exp_f32 v158, v158\n" \
    "v_exp_f32 v159, v159\n" \
    "s_setprio 2\n" \
    "s_waitcnt lgkmcnt(0)\n" \
    "v_mfma_f32_32x32x4_2b_bf16 v[96:127], v[234:235], v[232:233], v[160:191] cbsz:1 abid:0\n" \
    "v_add_u32 v86, 0xffff2000, v86\n" \
    "s_mov_b64 exec, s[48:49]\n" \
    "ds_read_b128 v[240:243], v86 offset:0\n" \
    "ds_read_b128 v[244:247], v86 offset:640\n" \
    "s_mov_b64 exec, -1\n" \
    "s_nop 3\n" \
    "v_smfmac_f32_32x32x32_bf16 v[192:207], v[248:251], v[128:135], v84\n" \
    "s_mul_i32 s45, %[lp], 81920\n" \
    "s_mul_i32 s66, %[head], 2560\n" \
    "s_add_u32 s66, s66, s45\n" \
    "v_smfmac_f32_32x32x32_bf16 v[192:207], v[252:255], v[136:143], v84\n" \
    "s_add_u32 s58, s40, s66\n" \
    "s_addc_u32 s59, s41, 0\n" \
    "s_add_u32 s60, s58, 2621440\n" \
    "s_addc_u32 s61, s59, 0\n" \
    "v_smfmac_f32_32x32x32_bf16 v[208:223], v[248:251], v[144:151], v84\n" \
    "v_mfma_f32_32x32x16_bf16 v[96:111], v[236:239], v[224:227], v[96:111]\n" \
    "s_cmp_gt_u32 %[lp], 0\n" \
    "s_cselect_b32 s51, 81664, 0\n" \
    "s_sub_u32 s62, s60, s51\n" \
    "s_subb_u32 s63, s61, 0\n" \
    "v_mfma_f32_32x32x16_bf16 v[112:127], v[236:239], v[228:231], v[112:127]\n" \
    "ds_read_b64 v[236:237], v85 offset:256\n" \
    "ds_read_b64 v[238:239], v85 offset:768\n" \
    "s_nop 2\n" \
    "v_smfmac_f32_32x32x32_bf16 v[208:223], v[252:255], v[152:159], v84\n" \
    "s_setprio 0\n" \
    "v_exp_f32 v96, v96\n" \
    "s_setprio 1\n" \
    "v_exp_f32 v97, v97\n" \
    "v_exp_f32 v98, v98\n" \
    "v_exp_f32 v99, v99\n" \
    "v_exp_f32 v100, v100\n" \
    "v_exp_f32 v101, v101\n" \
    "v_exp_f32 v102, v102\n" \
    "v_exp_f32 v103, v103\n" \
    "v_exp_f32 v104, v104\n" \
    "v_exp_f32 v105, v105\n" \
    "v_exp_f32 v106, v106\n" \
    "v_exp_f32 v107, v107\n" \
    "v_exp_f32 v108, v108\n" \
    "v_exp_f32 v109, v109\n" \
    "v_exp_f32 v110, v110\n" \
    "v_exp_f32 v111, v111\n" \
    "v_exp_f32 v112, v112\n" \
    "v_exp_f32 v113, v113\n" \
    "v_exp_f32 v114, v114\n" \
    "v_exp_f32 v115, v115\n" \
    "v_exp_f32 v116, v116\n" \
    "v_exp_f32 v117, v117\n" \
    "v_exp_f32 v118, v118\n" \
    "v_exp_f32 v119, v119\n" \
    "v_exp_f32 v120, v120\n" \
    "v_exp_f32 v121, v121\n" \
    "v_exp_f32 v122, v122\n" \
    "v_exp_f32 v123, v123\n" \
    "v_exp_f32 v124, v124\n" \
    "v_exp_f32 v125, v125\n" \
    "v_exp_f32 v126, v126\n" \
    "v_exp_f32 v127, v127\n" \
    "s_setprio 2\n" \
    "s_waitcnt lgkmcnt(0)\n" \
    "v_mfma_f32_32x32x4_2b_bf16 v[128:159], v[234:235], v[232:233], v[160:191] cbsz:1 abid:1\n" \
    "s_add_u32 s46, s46, 1\n" \
    "v_mov_b32 v95, s46\n" \
    "ds_write_b32 v93, v95\n" \
    "s_mov_b64 exec, s[48:49]\n" \
    "ds_read_b128 v[248:251], v86 offset:1280\n" \
    "ds_read_b128 v[252:255], v86 offset:1920\n" \
    "s_mov_b64 exec, -1\n" \
    "s_nop 1\n" \
    "v_smfmac_f32_32x32x32_bf16 v[192:207], v[240:243], v[96:103], v84\n" \
    "s_mul_i32 s66, %[head], 320\n" \
    "s_add_u32 s66, s66, s45\n" \
    "s_add_u32 s66, s66, 5242880\n" \
    "v_smfmac_f32_32x32x32_bf16 v[192:207], v[244:247], v[104:111], v84\n" \
    "s_add_u32 s54, s40, s66\n" \
    "s_addc_u32 s55, s41, 0\n" \
    "s_add_u32 s56, s40, %[wa]\n" \
    "s_addc_u32 s57, s41, 0\n" \
    "v_smfmac_f32_32x32x32_bf16 v[208:223], v[240:243], v[112:119], v84\n" \
    "v_mfma_f32_32x32x16_bf16 v[128:143], v[236:239], v[224:227], v[128:143]\n" \
    "s_lshr_b32 s66, %[wid], 2\n" \
    "s_mul_i32 s66, s66, 5120\n" \
    "s_add_u32 s47, s44, s66\n" \
    "s_add_u32 s47, s47, 98304\n" \
    "v_mfma_f32_32x32x16_bf16 v[144:159], v[236:239], v[228:231], v[144:159]\n" \
    "ds_read_b64 v[236:237], v85 offset:5120\n" \
    "ds_read_b64 v[238:239], v85 offset:5632\n" \
    "ds_read_b64 v[234:235], v85 offset:6144\n" \
    "s_nop 1\n" \
    "v_smfmac_f32_32x32x32_bf16 v[208:223], v[244:247], v[120:127], v84\n" \
    "s_setprio 0\n" \
    "v_exp_f32 v128, v128\n" \
    "s_setprio 1\n" \
    "v_exp_f32 v129, v129\n" \
    "v_exp_f32 v130, v130\n" \
    "v_exp_f32 v131, v131\n" \
    "v_exp_f32 v132, v132\n" \
    "v_exp_f32 v133, v133\n" \
    "v_exp_f32 v134, v134\n" \
    "v_exp_f32 v135, v135\n" \
    "v_exp_f32 v136, v136\n" \
    "v_exp_f32 v137, v137\n" \
    "v_exp_f32 v138, v138\n" \
    "v_exp_f32 v139, v139\n" \
    "v_exp_f32 v140, v140\n" \
    "v_exp_f32 v141, v141\n" \
    "v_exp_f32 v142, v142\n" \
    "v_exp_f32 v143, v143\n" \
    "v_exp_f32 v144, v144\n" \
    "v_exp_f32 v145, v145\n" \
    "v_exp_f32 v146, v146\n" \
    "v_exp_f32 v147, v147\n" \
    "v_exp_f32 v148, v148\n" \
    "v_exp_f32 v149, v149\n" \
    "v_exp_f32 v150, v150\n" \
    "v_exp_f32 v151, v151\n" \
    "v_exp_f32 v152, v152\n" \
    "v_exp_f32 v153, v153\n" \
    "v_exp_f32 v154, v154\n" \
    "v_exp_f32 v155, v155\n" \
    "v_exp_f32 v156, v156\n" \
    "v_exp_f32 v157, v157\n" \
    "v_exp_f32 v158, v158\n" \
    "v_exp_f32 v159, v159\n" \
    "s_setprio 2\n" \
    "s_waitcnt lgkmcnt(0)\n" \
    "v_mfma_f32_32x32x4_2b_bf16 v[96:127], v[234:235], v[232:233], v[160:191] cbsz:1 abid:0\n" \
    "ds_read_b32 v95, v94\n" \
    "s_mov_b64 exec, s[48:49]\n" \
    "ds_read_b128 v[240:243], v86 offset:5120\n" \
    "ds_read_b128 v[244:247], v86 offset:5760\n" \
    "s_mov_b64 exec, -1\n" \
    "s_nop 3\n" \
    "v_smfmac_f32_32x32x32_bf16 v[192:207], v[248:251], v[128:135], v84\n" \
    "s_waitcnt lgkmcnt(2)\n" \
    "v_cmp_gt_u32 vcc, s46, v95\n" \
    "s_movk_i32 s50, 0x4000\n" \
    "s_cbranch_vccnz .Latt_spin9\n" \
    ".Latt_go9:\n" \
    "v_smfmac_f32_32x32x32_bf16 v[192:207], v[252:255], v[136:143], v84\n" \
    "s_add_u32 m0, s44, 57344\n" \
    "s_nop 0\n" \
    "global_load_lds_dwordx4 v87, s[42:43] sc1\n" \
    "v_smfmac_f32_32x32x32_bf16 v[208:223], v[248:251], v[144:151], v84\n" \
    "s_add_u32 m0, s44, 58368\n" \
    "s_nop 0\n" \
    "global_load_lds_dwordx4 v88, s[42:43] sc1\n" \
    "v_mfma_f32_32x32x16_bf16 v[96:111], v[236:239], v[224:227], v[96:111]\n" \
    "s_add_u32 m0, s44, 59392\n" \
    "s_nop 0\n" \
    "global_load_lds_dwordx4 v89, s[42:43] sc1\n" \
    "s_nop 0\n" \
    "v_mfma_f32_32x32x16_bf16 v[112:127], v[236:239], v[228:231], v[112:127]\n" \
    "ds_read_b64 v[236:237], v85 offset:5376\n" \
    "ds_read_b64 v[238:239], v85 offset:5888\n" \
    "s_add_u32 m0, s44, 60416\n" \
    "s_nop 0\n" \
    "global_load_lds_dwordx4 v90, s[42:43] sc1\n" \
    "s_add_u32 m0, s44, 61440\n" \
    "s_nop 0\n" \
    "global_load_lds_dwordx4 v91, s[42:43] sc1\n" \
    "s_add_u32 s42, s42, 327680\n" \
    "s_addc_u32 s43, s43, 0\n" \
    "v_smfmac_f32_32x32x32_bf16 v[208:223], v[252:255], v[152:159], v84\n" \
    "s_setprio 0\n" \
    "v_exp_f32 v96, v96\n" \
    "s_setprio 1\n" \
    "v_exp_f32 v97, v97\n" \
    "v_exp_f32 v98, v98\n" \
    "v_exp_f32 v99, v99\n" \
    "v_exp_f32 v100, v100\n" \
    "v_exp_f32 v101, v101\n" \
    "v_exp_f32 v102, v102\n" \
    "v_exp_f32 v103, v103\n" \
    "v_exp_f32 v104, v104\n" \
    "v_exp_f32 v105, v105\n" \
    "v_exp_f32 v106, v106\n" \
    "v_exp_f32 v107, v107\n" \
    "v_exp_f32 v108, v108\n" \
    "v_exp_f32 v109, v109\n" \
    "v_exp_f32 v110, v110\n" \
    "v_exp_f32 v111, v111\n" \
    "v_exp_f32 v112, v112\n" \
    "v_exp_f32 v113, v113\n" \
    "v_exp_f32 v114, v114\n" \
    "v_exp_f32 v115, v115\n" \
    "v_exp_f32 v116, v116\n" \
    "v_exp_f32 v117, v117\n" \
    "v_exp_f32 v118, v118\n" \
    "v_exp_f32 v119, v119\n" \
    "v_exp_f32 v120, v120\n" \
    "v_exp_f32 v121, v121\n" \
    "v_exp_f32 v122, v122\n" \
    "v_exp_f32 v123, v123\n" \
    "v_exp_f32 v124, v124\n" \
    "v_exp_f32 v125, v125\n" \
    "v_exp_f32 v126, v126\n" \
    "v_exp_f32 v127, v127\n" \
    "s_setprio 2\n" \
    "s_waitcnt lgkmcnt(0)\n" \
    "v_mfma_f32_32x32x4_2b_bf16 v[128:159], v[234:235], v[232:233], v[160:191] cbsz:1 abid:1\n" \
    "s_mov_b64 exec, s[48:49]\n" \
    "ds_read_b128 v[248:251], v86 offset:6400\n" \
    "ds_read_b128 v[252:255], v86 offset:7040\n" \
    "s_mov_b64 exec, -1\n" \
    "s_nop 4\n" \
    "v_smfmac_f32_32x32x32_bf16 v[192:207], v[240:243], v[96:103], v84\n" \
    "global_load_dwordx2 v[34:35], v5, s[40:41] sc1\n" \
    "s_nop 1\n" \
    "v_smfmac_f32_32x32x32_bf16 v[192:207], v[244:247], v[104:111], v84\n" \
    "s_cmp_gt_u32 %[lp], 0\n" \
    "s_cselect_b32 s51, 20480, 0\n" \
    "s_sub_u32 s64, s54, s51\n" \
    "s_subb_u32 s65, s55, 0\n" \
    "v_smfmac_f32_32x32x32_bf16 v[208:223], v[240:243], v[112:119], v84\n" \
    "v_mfma_f32_32x32x16_bf16 v[128:143], v[236:239], v[224:227], v[128:143]\n" \
    "s_cmp_lt_u32 %[wid], 4\n" \
    "s_cselect_b64 s[52:53], -1, 0\n" \
    "s_nop 1\n" \
    "v_mfma_f32_32x32x16_bf16 v[144:159], v[236:239], v[228:231], v[144:159]\n" \
    "ds_read_b64 v[236:237], v85 offset:10240\n" \
    "ds_read_b64 v[238:239], v85 offset:10752\n" \
    "ds_read_b64 v[234:235], v85 offset:11264\n" \
    "s_nop 1\n" \
    "v_smfmac_f32_32x32x32_bf16 v[208:223], v[244:247], v[120:127], v84\n" \
    "s_setprio 0\n" \
    "v_exp_f32 v128, v128\n" \
    "s_setprio 1\n" \
    "v_exp_f32 v129, v129\n" \
    "v_exp_f32 v130, v130\n" \
    "v_exp_f32 v131, v131\n" \
    "v_exp_f32 v132, v132\n" \
    "v_exp_f32 v133, v133\n" \
    "v_exp_f32 v134, v134\n" \
    "v_exp_f32 v135, v135\n" \
    "v_exp_f32 v136, v136\n" \
    "v_exp_f32 v137, v137\n" \
    "v_exp_f32 v138, v138\n" \
    "v_exp_f32 v139, v139\n" \
    "v_exp_f32 v140, v140\n" \
    "v_exp_f32 v141, v141\n" \
    "v_exp_f32 v142, v142\n" \
    "v_exp_f32 v143, v143\n" \
    "v_exp_f32 v144, v144\n" \
    "v_exp_f32 v145, v145\n" \
    "v_exp_f32 v146, v146\n" \
    "v_exp_f32 v147, v147\n" \
    "v_exp_f32 v148, v148\n" \
    "v_exp_f32 v149, v149\n" \
    "v_exp_f32 v150, v150\n" \
    "v_exp_f32 v151, v151\n" \
    "v_exp_f32 v152, v152\n" \
    "v_exp_f32 v153, v153\n" \
    "v_exp_f32 v154, v154\n" \
    "v_exp_f32 v155, v155\n" \
    "v_exp_f32 v156, v156\n" \
    "v_exp_f32 v157, v157\n" \
    "v_exp_f32 v158, v158\n" \
    "v_exp_f32 v159, v159\n" \
    "s_setprio 2\n" \
    "s_waitcnt lgkmcnt(0)\n" \
    "v_mfma_f32_32x32x4_2b_bf16 v[96:127], v[234:235], v[232:233], v[160:191] cbsz:1 abid:0\n" \
    "s_add_u32 s46, s46, 1\n" \
    "s_waitcnt vmcnt(0)\n" \
    "v_mov_b32 v95, s46\n" \
    "ds_write_b32 v93, v95\n" \
    "v_cmp_ne_u64 vcc, %[tag], v[34:35]\n" \
    "s_mov_b32 s50, 0x10000\n" \
    "s_cbranch_vccnz .Latt_fspin11\n" \
    ".Latt_fgo11:\n" \
    "s_mov_b64 exec, s[48:49]\n" \
    "ds_read_b128 v[240:243], v86 offset:10240\n" \
    "ds_read_b128 v[244:247], v86 offset:10880\n" \
    "s_mov_b64 exec, -1\n" \
    "v_smfmac_f32_32x32x32_bf16 v[192:207], v[248:251], v[128:135], v84\n" \
    "s_mov_b64 exec, s[52:53]\n" \
    "global_load_dwordx2 v[76:77], v2, s[62:63] offset:2048 sc1\n" \
    "s_mov_b64 exec, -1\n" \
    "v_smfmac_f32_32x32x32_bf16 v[192:207], v[252:255], v[136:143], v84\n" \
    "s_mov_b64 exec, s[52:53]\n" \
    "global_load_dwordx2 v[80:81], v1, s[58:59] offset:2048 sc1\n" \
    "s_mov_b64 exec, -1\n" \
    "v_smfmac_f32_32x32x32_bf16 v[208:223], v[248:251], v[144:151], v84\n" \
    "s_mov_b64 exec, s[52:53]\n" \
    "global_load_dwordx2 v[56:57], v0, s[62:63] offset:0 sc1\n" \
    "s_mov_b64 exec, -1\n" \
    "v_mfma_f32_32x32x16_bf16 v[96:111], v[236:239], v[224:227], v[96:111]\n" \
    "s_mov_b64 exec, s[52:53]\n" \
    "global_load_dwordx2 v[58:59], v0, s[62:63] offset:512 sc1\n" \
    "s_mov_b64 exec, -1\n" \
    "s_nop 0\n" \
    "v_mfma_f32_32x32x16_bf16 v[112:127], v[236:239], v[228:231], v[112:127]\n" \
    "ds_read_b64 v[236:237], v85 offset:10496\n" \
    "ds_read_b64 v[238:239], v85 offset:11008\n" \
    "s_mov_b64 exec, s[52:53]\n" \
    "global_load_dwordx2 v[60:61], v0, s[60:61] offset:0 sc1\n" \
    "s_mov_b64 exec, -1\n" \
    "v_smfmac_f32_32x32x32_bf16 v[208:223], v[252:255], v[152:159], v84\n" \
    "s_setprio 0\n" \
    "v_exp_f32 v96, v96\n" \
    "s_setprio 1\n" \
    "v_exp_f32 v97, v97\n" \
    "v_exp_f32 v98, v98\n" \
    "v_exp_f32 v99, v99\n" \
    "v_exp_f32 v100, v100\n" \
    "v_exp_f32 v101, v101\n" \
    "v_exp_f32 v102, v102\n" \
    "v_exp_f32 v103, v103\n" \
    "v_exp_f32 v104, v104\n" \
    "v_exp_f32 v105, v105\n" \
    "v_exp_f32 v106, v106\n" \
    "v_exp_f32 v107, v107\n" \
    "v_exp_f32 v108, v108\n" \
    "v_exp_f32 v109, v109\n" \
    "v_exp_f32 v110, v110\n" \
    "v_exp_f32 v111, v111\n" \
    "v_exp_f32 v112, v112\n" \
    "v_exp_f32 v113, v113\n" \
    "v_exp_f32 v114, v114\n" \
    "v_exp_f32 v115, v115\n" \
    "v_exp_f32 v116, v116\n" \
    "v_exp_f32 v117, v117\n" \
    "v_exp_f32 v118, v118\n" \
    "v_exp_f32 v119, v119\n" \
    "v_exp_f32 v120, v120\n" \
    "v_exp_f32 v121, v121\n" \
    "v_exp_f32 v122, v122\n" \
    "v_exp_f32 v123, v123\n" \
    "v_exp_f32 v124, v124\n" \
    "v_exp_f32 v125, v125\n" \
    "v_exp_f32 v126, v126\n" \
    "v_exp_f32 v127, v127\n" \
    "s_setprio 2\n" \
    "s_waitcnt lgkmcnt(0)\n" \
    "v_mfma_f32_32x32x4_2b_bf16 v[128:159], v[234:235], v[232:233], v[160:191] cbsz:1 abid:1\n" \
    "ds_read_b32 v95, v94\n" \
    "s_mov_b64 exec, s[48:49]\n" \
    "ds_read_b128 v[248:251], v86 offset:11520\n" \
    "ds_read_b128 v[252:255], v86 offset:12160\n" \
    "s_mov_b64 exec, -1\n" \
    "s_nop 3\n" \
    "v_smfmac_f32_32x32x32_bf16 v[192:207], v[240:243], v[96:103], v84\n" \
    "s_waitcnt lgkmcnt(2)\n" \
    "v_cmp_gt_u32 vcc, s46, v95\n" \
    "s_movk_i32 s50, 0x4000\n" \
    "s_cbranch_vccnz .Latt_spin12\n" \
    ".Latt_go12:\n" \
    "v_smfmac_f32_32x32x32_bf16 v[192:207], v[244:247], v[104:111], v84\n" \
    "s_mov_b64 exec, s[52:53]\n" \
    "global_load_dwordx2 v[62:63], v0, s[60:61] offset:512 sc1\n" \
    "s_mov_b64 exec, -1\n" \
    "v_smfmac_f32_32x32x32_bf16 v[208:223], v[240:243], v[112:119], v84\n" \
    "s_mov_b64 exec, s[52:53]\n" \
    "global_load_dwordx2 v[68:69], v0, s[58:59] offset:0 sc1\n" \
    "s_mov_b64 exec, -1\n" \
    "v_mfma_f32_32x32x16_bf16 v[128:143], v[236:239], v[224:227], v[128:143]\n" \
    "s_mov_b64 exec, s[52:53]\n" \
    "global_load_dwordx2 v[70:71], v0, s[58:59] offset:512 sc1\n" \
    "s_mov_b64 exec, -1\n" \
    "s_nop 0\n" \
    "v_mfma_f32_32x32x16_bf16 v[144:159], v[236:239], v[228:231], v[144:159]\n" \
    "ds_read_b64 v[236:237], v85 offset:15360\n" \
    "ds_read_b64 v[238:239], v85 offset:15872\n" \
    "ds_read_b64 v[234:235], v85 offset:16384\n" \
    "s_mov_b64 exec, s[52:53]\n" \
    "global_load_dwordx2 v[78:79], v3, s[60:61] offset:2048 sc1\n" \
    "s_mov_b64 exec, -1\n" \
    "v_smfmac_f32_32x32x32_bf16 v[208:223], v[244:247], v[120:127], v84\n" \
    "s_setprio 0\n" \
    "v_exp_f32 v128, v128\n" \
    "s_setprio 1\n" \
    "v_exp_f32 v129, v129\n" \
    "v_exp_f32 v130, v130\n" \
    "v_exp_f32 v131, v131\n" \
    "v_exp_f32 v132, v132\n" \
    "v_exp_f32 v133, v133\n" \
    "v_exp_f32 v134, v134\n" \
    "v_exp_f32 v135, v135\n" \
    "v_exp_f32 v136, v136\n" \
    "v_exp_f32 v137, v137\n" \
    "v_exp_f32 v138, v138\n" \
    "v_exp_f32 v139, v139\n" \
    "v_exp_f32 v140, v140\n" \
    "v_exp_f32 v141, v141\n" \
    "v_exp_f32 v142, v142\n" \
    "v_exp_f32 v143, v143\n" \
    "v_exp_f32 v144, v144\n" \
    "v_exp_f32 v145, v145\n" \
    "v_exp_f32 v146, v146\n" \
    "v_exp_f32 v147, v147\n" \
    "v_exp_f32 v148, v148\n" \
    "v_exp_f32 v149, v149\n" \
    "v_exp_f32 v150, v150\n" \
    "v_exp_f32 v151, v151\n" \
    "v_exp_f32 v152, v152\n" \
    "v_exp_f32 v153, v153\n" \
    "v_exp_f32 v154, v154\n" \
    "v_exp_f32 v155, v155\n" \
    "v_exp_f32 v156, v156\n" \
    "v_exp_f32 v157, v157\n" \
    "v_exp_f32 v158, v158\n" \
    "v_exp_f32 v159, v159\n" \
    "s_setprio 2\n" \
    "s_waitcnt lgkmcnt(0)\n" \
    "v_mfma_f32_32x32x4_2b_bf16 v[96:127], v[234:235], v[232:233], v[160:191] cbsz:1 abid:0\n" \
    "s_mov_b64 exec, s[48:49]\n" \
    "ds_read_b128 v[240:243], v86 offset:15360\n" \
    "ds_read_b128 v[244:247], v86 offset:16000\n" \
    "s_mov_b64 exec, -1\n" \
    "s_nop 4\n" \
    "v_smfmac_f32_32x32x32_bf16 v[192:207], v[248:251], v[128:135], v84\n" \
    "s_mov_b64 exec, s[52:53]\n" \
    "global_load_dwordx2 v[82:83], v1, s[58:59] offset:2304 sc1\n" \
    "s_mov_b64 exec, -1\n" \
    "v_smfmac_f32_32x32x32_bf16 v[192:207], v[252:255], v[136:143], v84\n" \
    "s_mov_b64 exec, s[52:53]\n" \
    "global_load_dwordx2 v[64:65], v0, s[60:61] offset:256 sc1\n" \
    "s_mov_b64 exec, -1\n" \
    "v_smfmac_f32_32x32x32_bf16 v[208:223], v[248:251], v[144:151], v84\n" \
    "s_mov_b64 exec, s[52:53]\n" \
    "global_load_dwordx2 v[66:67], v0, s[60:61] offset:768 sc1\n" \
    "s_mov_b64 exec, -1\n" \
    "v_mfma_f32_32x32x16_bf16 v[96:111], v[236:239], v[224:227], v[96:111]\n" \
    "s_mov_b64 exec, s[52:53]\n" \
    "global_load_dwordx2 v[72:73], v0, s[58:59] offset:256 sc1\n" \
    "s_mov_b64 exec, -1\n" \
    "s_nop 0\n" \
    "v_mfma_f32_32x32x16_bf16 v[112:127], v[236:239], v[228:231], v[112:127]\n" \
    "ds_read_b64 v[236:237], v85 offset:15616\n" \
    "ds_read_b64 v[238:239], v85 offset:16128\n" \
    "s_mov_b64 exec, s[52:53]\n" \
    "global_load_dwordx2 v[74:75], v0, s[58:59] offset:768 sc1\n" \
    "s_mov_b64 exec, -1\n" \
    "s_and_b64 s[52:53], s[52:53], s[48:49]\n" \
    "v_smfmac_f32_32x32x32_bf16 v[208:223], v[252:255], v[152:159], v84\n" \
    "s_setprio 0\n" \
    "v_exp_f32 v96, v96\n" \
    "s_setprio 1\n" \
    "v_exp_f32 v97, v97\n" \
    "v_exp_f32 v98, v98\n" \
    "v_exp_f32 v99, v99\n" \
    "v_exp_f32 v100, v100\n" \
    "v_exp_f32 v101, v101\n" \
    "v_exp_f32 v102, v102\n" \
    "v_exp_f32 v103, v103\n" \
    "v_exp_f32 v104, v104\n" \
    "v_exp_f32 v105, v105\n" \
    "v_exp_f32 v106, v106\n" \
    "v_exp_f32 v107, v107\n" \
    "v_exp_f32 v108, v108\n" \
    "v_exp_f32 v109, v109\n" \
    "v_exp_f32 v110, v110\n" \
    "v_exp_f32 v111, v111\n" \
    "v_exp_f32 v112, v112\n" \
    "v_exp_f32 v113, v113\n" \
    "v_exp_f32 v114, v114\n" \
    "v_exp_f32 v115, v115\n" \
    "v_exp_f32 v116, v116\n" \
    "v_exp_f32 v117, v117\n" \
    "v_exp_f32 v118, v118\n" \
    "v_exp_f32 v119, v119\n" \
    "v_exp_f32 v120, v120\n" \
    "v_exp_f32 v121, v121\n" \
    "v_exp_f32 v122, v122\n" \
    "v_exp_f32 v123, v123\n" \
    "v_exp_f32 v124, v124\n" \
    "v_exp_f32 v125, v125\n" \
    "v_exp_f32 v126, v126\n" \
    "v_exp_f32 v127, v127\n" \
    "s_setprio 2\n" \
    "s_waitcnt lgkmcnt(0)\n" \
    "v_mfma_f32_32x32x4_2b_bf16 v[128:159], v[234:235], v[232:233], v[160:191] cbsz:1 abid:1\n" \
    "v_add_u32 v85, 0xe000, v85\n" \
    "s_mov_b64 exec, s[48:49]\n" \
    "ds_read_b128 v[248:251], v86 offset:16640\n" \
    "ds_read_b128 v[252:255], v86 offset:17280\n" \
    "s_mov_b64 exec, -1\n" \
    "s_nop 3\n" \
    "v_smfmac_f32_32x32x32_bf16 v[192:207], v[240:243], v[96:103], v84\n" \
    "s_mov_b64 exec, s[52:53]\n" \
    "global_load_dwordx4 v[36:39], v4, s[64:65] sc1\n" \
    "s_mov_b64 exec, -1\n" \
    "v_smfmac_f32_32x32x32_bf16 v[192:207], v[244:247], v[104:111], v84\n" \
    "s_mov_b64 exec, s[52:53]\n" \
    "global_load_dwordx4 v[40:43], v4, s[54:55] sc1\n" \
    "s_mov_b64 exec, -1\n" \
    "s_add_u32 s54, s54, 20480\n" \
    "s_addc_u32 s55, s55, 0\n" \
    "v_smfmac_f32_32x32x32_bf16 v[208:223], v[240:243], v[112:119], v84\n" \
    "s_mov_b64 exec, s[52:53]\n" \
    "global_load_dwordx4 v[44:47], v4, s[54:55] sc1\n" \
    "s_mov_b64 exec, -1\n" \
    "s_add_u32 s54, s54, 20480\n" \
    "s_addc_u32 s55, s55, 0\n" \
    "v_mfma_f32_32x32x16_bf16 v[128:143], v[236:239], v[224:227], v[128:143]\n" \
    "s_mov_b64 exec, s[52:53]\n" \
    "global_load_dwordx4 v[48:51], v4, s[54:55] sc1\n" \
    "s_mov_b64 exec, -1\n" \
    "s_add_u32 s54, s54, 20480\n" \
    "s_addc_u32 s55, s55, 0\n" \
    "v_mfma_f32_32x32x16_bf16 v[144:159], v[236:239], v[228:231], v[144:159]\n" \
    "ds_read_b64 v[236:237], v85 offset:0\n" \
    "ds_read_b64 v[238:239], v85 offset:512\n" \
    "ds_read_b64 v[234:235], v85 offset:1024\n" \
    "s_mov_b64 exec, s[52:53]\n" \
    "global_load_dwordx4 v[52:55], v4, s[54:55] sc1\n" \
    "s_mov_b64 exec, -1\n" \
    "s_add_u32 s54, s54, 20480\n" \
    "s_addc_u32 s55, s55, 0\n" \
    "v_smfmac_f32_32x32x32_bf16 v[208:223], v[244:247], v[120:127], v84\n" \
    "s_setprio 0\n" \
    "v_exp_f32 v128, v128\n" \
    "s_setprio 1\n" \
    "v_exp_f32 v129, v129\n" \
    "v_exp_f32 v130, v130\n" \
    "v_exp_f32 v131, v131\n" \
    "v_exp_f32 v132, v132\n" \
    "v_exp_f32 v133, v133\n" \
    "v_exp_f32 v134, v134\n" \
    "v_exp_f32 v135, v135\n" \
    "v_exp_f32 v136, v136\n" \
    "v_exp_f32 v137, v137\n" \
    "v_exp_f32 v138, v138\n" \
    "v_exp_f32 v139, v139\n" \
    "v_exp_f32 v140, v140\n" \
    "v_exp_f32 v141, v141\n" \
    "v_exp_f32 v142, v142\n" \
    "v_exp_f32 v143, v143\n" \
    "v_exp_f32 v144, v144\n" \
    "v_exp_f32 v145, v145\n" \
    "v_exp_f32 v146, v146\n" \
    "v_exp_f32 v147, v147\n" \
    "v_exp_f32 v148, v148\n" \
    "v_exp_f32 v149, v149\n" \
    "v_exp_f32 v150, v150\n" \
    "v_exp_f32 v151, v151\n" \
    "v_exp_f32 v152, v152\n" \
    "v_exp_f32 v153, v153\n" \
    "v_exp_f32 v154, v154\n" \
    "v_exp_f32 v155, v155\n" \
    "v_exp_f32 v156, v156\n" \
    "v_exp_f32 v157, v157\n" \
    "v_exp_f32 v158, v158\n" \
    "v_exp_f32 v159, v159\n" \
    "s_setprio 2\n" \
    "s_waitcnt lgkmcnt(0)\n" \
    "v_mfma_f32_32x32x4_2b_bf16 v[96:127], v[234:235], v[232:233], v[160:191] cbsz:1 abid:0\n" \
    "v_add_u32 v86, 0xe000, v86\n" \
    "s_mov_b64 exec, s[48:49]\n" \
    "ds_read_b128 v[240:243], v86 offset:0\n" \
    "ds_read_b128 v[244:247], v86 offset:640\n" \
    "s_mov_b64 exec, -1\n" \
    "s_nop 3\n" \
    "v_smfmac_f32_32x32x32_bf16 v[192:207], v[248:251], v[128:135], v84\n" \
    "s_add_u32 m0, s47, 0\n" \
    "s_nop 0\n" \
    "global_load_lds_dwordx4 v92, s[56:57] sc1\n" \
    "s_add_u32 s56, s56, 1024\n" \
    "s_addc_u32 s57, s57, 0\n" \
    "v_smfmac_f32_32x32x32_bf16 v[192:207], v[252:255], v[136:143], v84\n" \
    "s_add_u32 m0, s47, 1024\n" \
    "s_nop 0\n" \
    "global_load_lds_dwordx4 v92, s[56:57] sc1\n" \
    "s_add_u32 s56, s56, 1024\n" \
    "s_addc_u32 s57, s57, 0\n" \
    "v_smfmac_f32_32x32x32_bf16 v[208:223], v[248:251], v[144:151], v84\n" \
    "s_add_u32 m0, s47, 2048\n" \
    "s_nop 0\n" \
    "global_load_lds_dwordx4 v92, s[56:57] sc1\n" \
    "s_add_u32 s56, s56, 1024\n" \
    "s_addc_u32 s57, s57, 0\n" \
    "v_mfma_f32_32x32x16_bf16 v[96:111], v[236:239], v[224:227], v[96:111]\n" \
    "s_add_u32 m0, s47, 3072\n" \
    "s_nop 0\n" \
    "global_load_lds_dwordx4 v92, s[56:57] sc1\n" \
    "s_add_u32 s56, s56, 1024\n" \
    "s_addc_u32 s57, s57, 0\n" \
    "v_mfma_f32_32x32x16_bf16 v[112:127], v[236:239], v[228:231], v[112:127]\n" \
    "ds_read_b64 v[236:237], v85 offset:256\n" \
    "ds_read_b64 v[238:239], v85 offset:768\n" \
    "s_add_u32 m0, s47, 4096\n" \
    "s_nop 0\n" \
    "global_load_lds_dwordx4 v92, s[56:57] sc1\n" \
    "s_add_u32 s56, s56, 1024\n" \
    "s_addc_u32 s57, s57, 0\n" \
    "v_smfmac_f32_32x32x32_bf16 v[208:223], v[252:255], v[152:159], v84\n" \
    "s_waitcnt vmcnt(5)\n" \
    "s_cmp_lt_u32 %[wid], 4\n" \
    "s_cbranch_scc1 .Latt_loc\n" \
    "s_add_u32 s66, %[b], %[wid]\n" \
    "s_cmp_eq_u32 s66, 262\n" \
    "s_cbranch_scc1 .Latt_ret\n" \
    "s_cmp_ge_u32 %[wid], 6\n" \
    "s_cbranch_scc1 .Latt_b\n" \
    "v_mov_b32 v34, 0\n" \
    "v_dot2c_f32_bf16 v34, v36, v56\n" \
    "v_dot2c_f32_bf16 v34, v37, v57\n" \
    "v_dot2c_f32_bf16 v34, v38, v58\n" \
    "v_dot2c_f32_bf16 v34, v39, v59\n" \
    "v_dot2c_f32_bf16 v34, v40, v60\n" \
    "v_dot2c_f32_bf16 v34, v41, v61\n" \
    "v_dot2c_f32_bf16 v34, v42, v62\n" \
    "v_dot2c_f32_bf16 v34, v43, v63\n" \
    "v_dot2c_f32_bf16 v34, v44, v64\n" \
    "v_dot2c_f32_bf16 v34, v45, v65\n" \
    "v_dot2c_f32_bf16 v34, v46, v66\n" \
    "v_dot2c_f32_bf16 v34, v47, v67\n" \
    "v_dot2c_f32_bf16 v34, v48, v68\n" \
    "v_dot2c_f32_bf16 v34, v49, v69\n" \
    "v_dot2c_f32_bf16 v34, v50, v70\n" \
    "v_dot2c_f32_bf16 v34, v51, v71\n" \
    "v_dot2c_f32_bf16 v34, v52, v72\n" \
    "v_dot2c_f32_bf16 v34, v53, v73\n" \
    "v_dot2c_f32_bf16 v34, v54, v74\n" \
    "v_dot2c_f32_bf16 v34, v55, v75\n" \
    "s_lshr_b32 s66, %[b], 5\n" \
    "s_lshl_b32 s66, s66, 5\n" \
    "s_add_u32 s66, s66, %[head]\n" \
    "s_lshl_b32 s66, s66, 1\n" \
    "s_and_b32 s67, %[wid], 1\n" \
    "s_add_u32 s66, s66, s67\n" \
    "s_lshl_b32 s66, s66, 4\n" \
    "s_add_u32 s66, s66, 22609920\n" \
    "s_add_u32 s58, s40, s66\n" \
    "s_addc_u32 s59, s41, 0\n" \
    "s_nop 1\n" \
    "v_add_f32_dpp v34, v34, v34 row_ror:8 row_mask:0xf bank_mask:0xf\n" \
    "s_nop 1\n" \
    "v_add_f32_dpp v34, v34, v34 row_ror:4 row_mask:0xf bank_mask:0xf\n" \
    "s_nop 1\n" \
    "v_add_f32_dpp v34, v34, v34 row_ror:2 row_mask:0xf bank_mask:0xf\n" \
    "s_nop 1\n" \
    "v_add_f32_dpp v34, v34, v34 row_ror:1 row_mask:0xf bank_mask:0xf\n" \
    "v_mul_f32 v34, 0xbfb8aa3b, v34\n" \
    "v_exp_f32 v34, v34\n" \
    "v_lshrrev_b32 v35, 6, v92\n" \
    "v_add_f32 v34, 1.0, v34\n" \
    "v_rcp_f32 v34, v34\n" \
    "s_mov_b32 s52, 0x10001\n" \
    "s_mov_b32 s53, s52\n" \
    "s_mov_b64 exec, s[52:53]\n" \
    "global_store_dword v35, v34, s[58:59] sc1\n" \
    "s_mov_b64 exec, -1\n" \
    "s_branch .Latt_b\n" \
    ".Latt_loc:\n" \
    "s_setprio 3\n" \
    "v_add_u32 v95, v0, v1\n" \
    "v_mfma_f32_32x32x4_2b_bf16 v[128:159], v[76:77], v[80:81], v[0:31]\n" \
    "v_mfma_f32_32x32x4_2b_bf16 v[0:31], v[78:79], v[82:83], v[0:31]\n" \
    "s_lshr_b32 s66, %[head], 1\n" \
    "s_mul_i32 s66, s66, 5120\n" \
    "s_add_u32 s66, s66, s45\n" \
    "s_add_u32 s66, s66, 15728640\n" \
    "s_and_b32 s67, %[head], 1\n" \
    "s_lshl_b32 s51, s67, 11\n" \
    "s_add_u32 s51, s51, s66\n" \
    "s_add_u32 s58, s40, s51\n" \
    "s_addc_u32 s59, s41, 0\n" \
    "s_lshl_b32 s67, s67, 3\n" \
    "s_add_u32 s66, s66, s67\n" \
    "s_add_u32 s66, s66, 4096\n" \
    "s_add_u32 s60, s40, s66\n" \
    "s_addc_u32 s61, s41, 0\n" \
    "s_nop 3\n" \
    "v_mfma_f32_32x32x16_bf16 v[128:143], v[56:59], v[68:71], v[128:143]\n" \
    "v_mfma_f32_32x32x16_bf16 v[144:159], v[60:63], v[68:71], v[144:159]\n" \
    "v_mfma_f32_32x32x16_bf16 v[0:15], v[60:63], v[72:75], v[0:15]\n" \
    "v_mfma_f32_32x32x16_bf16 v[16:31], v[64:67], v[72:75], v[16:31]\n" \
    "s_cmp_lg_u32 %[lp], 0\n" \
    "s_nop 8\n" \
    "s_cbranch_scc1 1f\n" \
    "v_mov_b32 v136, 0xff61b1e6\n" \
    "v_mov_b32 v137, 0xff61b1e6\n" \
    "v_mov_b32 v138, 0xff61b1e6\n" \
    "v_mov_b32 v139, 0xff61b1e6\n" \
    "v_mov_b32 v140, 0xff61b1e6\n" \
    "v_mov_b32 v141, 0xff61b1e6\n" \
    "v_mov_b32 v142, 0xff61b1e6\n" \
    "v_mov_b32 v143, 0xff61b1e6\n" \
    "1:\n" \
    "v_mov_b64 v[56:57], 0\n" \
    "v_mov_b64 v[58:59], 0\n" \
    "v_mov_b64 v[60:61], 0\n" \
    "v_mov_b64 v[62:63], 0\n" \
    "v_mov_b64 v[64:65], 0\n" \
    "v_mov_b64 v[66:67], 0\n" \
    "v_mov_b64 v[68:69], 0\n" \
    "v_mov_b64 v[70:71], 0\n" \
    "v_max3_f32 v34, v136, v137, v138\n" \
    "v_max3_f32 v34, v34, v139, v140\n" \
    "v_max3_f32 v34, v34, v141, v142\n" \
    "v_max3_f32 v34, v34, v143, v144\n" \
    "v_max3_f32 v34, v34, v145, v146\n" \
    "v_max3_f32 v34, v34, v147, v148\n" \
    "v_max3_f32 v34, v34, v149, v150\n" \
    "v_max3_f32 v34, v34, v151, v152\n" \
    "v_max3_f32 v34, v34, v153, v154\n" \
    "v_max3_f32 v34, v34, v155, v156\n" \
    "v_max3_f32 v34, v34, v157, v158\n" \
    "v_max_f32 v34, v34, v159\n" \
    "v_mov_b32 v35, v34\n" \
    "s_nop 1\n" \
    "v_permlane32_swap_b32 v34, v35\n" \
    "v_max_f32 v34, v34, v35\n" \
    "v_sub_f32 v136, v136, v34\n" \
    "v_sub_f32 v137, v137, v34\n" \
    "v_sub_f32 v138, v138, v34\n" \
    "v_sub_f32 v139, v139, v34\n" \
    "v_sub_f32 v140, v140, v34\n" \
    "v_sub_f32 v141, v141, v34\n" \
    "v_sub_f32 v142, v142, v34\n" \
    "v_sub_f32 v143, v143, v34\n" \
    "v_sub_f32 v144, v144, v34\n" \
    "v_sub_f32 v145, v145, v34\n" \
    "v_sub_f32 v146, v146, v34\n" \
    "v_sub_f32 v147, v147, v34\n" \
    "v_sub_f32 v148, v148, v34\n" \
    "v_sub_f32 v149, v149, v34\n" \
    "v_sub_f32 v150, v150, v34\n" \
    "v_sub_f32 v151, v151, v34\n" \
    "v_sub_f32 v152, v152, v34\n" \
    "v_sub_f32 v153, v153, v34\n" \
    "v_sub_f32 v154, v154, v34\n" \
    "v_sub_f32 v155, v155, v34\n" \
    "v_sub_f32 v156, v156, v34\n" \
    "v_sub_f32 v157, v157, v34\n" \
    "v_sub_f32 v158, v158, v34\n" \
    "v_sub_f32 v159, v159, v34\n" \
    "v_exp_f32 v136, v136\n" \
    "v_exp_f32 v137, v137\n" \
    "v_exp_f32 v138, v138\n" \
    "v_exp_f32 v139, v139\n" \
    "v_exp_f32 v140, v140\n" \
    "v_exp_f32 v141, v141\n" \
    "v_exp_f32 v142, v142\n" \
    "v_exp_f32 v143, v143\n" \
    "v_exp_f32 v144, v144\n" \
    "v_exp_f32 v145, v145\n" \
    "v_exp_f32 v146, v146\n" \
    "v_exp_f32 v147, v147\n" \
    "v_exp_f32 v148, v148\n" \
    "v_exp_f32 v149, v149\n" \
    "v_exp_f32 v150, v150\n" \
    "v_exp_f32 v151, v151\n" \
    "v_exp_f32 v152, v152\n" \
    "v_exp_f32 v153, v153\n" \
    "v_exp_f32 v154, v154\n" \
    "v_exp_f32 v155, v155\n" \
    "v_exp_f32 v156, v156\n" \
    "v_exp_f32 v157, v157\n" \
    "v_exp_f32 v158, v158\n" \
    "v_exp_f32 v159, v159\n" \
    "s_nop 1\n" \
    "v_smfmac_f32_32x32x32_bf16 v[56:71], v[36:39], v[136:143], v84\n" \
    "v_smfmac_f32_32x32x32_bf16 v[56:71], v[40:43], v[144:151], v84\n" \
    "v_smfmac_f32_32x32x32_bf16 v[56:71], v[44:47], v[152:159], v84\n" \
    ".Lis2: s_cbranch_execz .Lis_ret\n" \
    "s_setprio 0\n" \
    "v_max3_f32 v34, v8, v9, v10\n" \
    "v_max3_f32 v34, v34, v11, v12\n" \
    "v_max3_f32 v34, v34, v13, v14\n" \
    "v_max3_f32 v34, v34, v15, v16\n" \
    "v_max3_f32 v34, v34, v17, v18\n" \
    "v_max3_f32 v34, v34, v19, v20\n" \
    "v_max3_f32 v34, v34, v21, v22\n" \
    "v_max3_f32 v34, v34, v23, v24\n" \
    "v_max3_f32 v34, v34, v25, v26\n" \
    "v_max3_f32 v34, v34, v27, v28\n" \
    "v_max3_f32 v34, v34, v29, v30\n" \
    "v_max_f32 v34, v34, v31\n" \
    "v_mov_b32 v35, v34\n" \
    "s_nop 1\n" \
    "v_permlane32_swap_b32 v34, v35\n" \
    "v_max_f32 v34, v34, v35\n" \
    "v_sub_f32 v8, v8, v34\n" \
    "v_sub_f32 v9, v9, v34\n" \
    "v_sub_f32 v10, v10, v34\n" \
    "v_sub_f32 v11, v11, v34\n" \
    "v_sub_f32 v12, v12, v34\n" \
    "v_sub_f32 v13, v13, v34\n" \
    "v_sub_f32 v14, v14, v34\n" \
    "v_sub_f32 v15, v15, v34\n" \
    "v_sub_f32 v16, v16, v34\n" \
    "v_sub_f32 v17, v17, v34\n" \
    "v_sub_f32 v18, v18, v34\n" \
    "v_sub_f32 v19, v19, v34\n" \
    "v_sub_f32 v20, v20, v34\n" \
    "v_sub_f32 v21, v21, v34\n" \
    "v_sub_f32 v22, v22, v34\n" \
    "v_sub_f32 v23, v23, v34\n" \
    "v_sub_f32 v24, v24, v34\n" \
    "v_sub_f32 v25, v25, v34\n" \
    "v_sub_f32 v26, v26, v34\n" \
    "v_sub_f32 v27, v27, v34\n" \
    "v_sub_f32 v28, v28, v34\n" \
    "v_sub_f32 v29, v29, v34\n" \
    "v_sub_f32 v30, v30, v34\n" \
    "v_sub_f32 v31, v31, v34\n" \
    "v_exp_f32 v8, v8\n" \
    "v_exp_f32 v9, v9\n" \
    "v_exp_f32 v10, v10\n" \
    "v_exp_f32 v11, v11\n" \
    "v_exp_f32 v12, v12\n" \
    "v_exp_f32 v13, v13\n" \
    "v_exp_f32 v14, v14\n" \
    "v_exp_f32 v15, v15\n" \
    "v_exp_f32 v16, v16\n" \
    "v_exp_f32 v17, v17\n" \
    "v_exp_f32 v18, v18\n" \
    "v_exp_f32 v19, v19\n" \
    "v_exp_f32 v20, v20\n" \
    "v_exp_f32 v21, v21\n" \
    "v_exp_f32 v22, v22\n" \
    "v_exp_f32 v23, v23\n" \
    "v_exp_f32 v24, v24\n" \
    "v_exp_f32 v25, v25\n" \
    "v_exp_f32 v26, v26\n" \
    "v_exp_f32 v27, v27\n" \
    "v_exp_f32 v28, v28\n" \
    "v_exp_f32 v29, v29\n" \
    "v_exp_f32 v30, v30\n" \
    "v_exp_f32 v31, v31\n" \
    "v_mov_b64 v[128:129], 0\n" \
    "v_mov_b64 v[130:131], 0\n" \
    "v_mov_b64 v[132:133], 0\n" \
    "v_mov_b64 v[134:135], 0\n" \
    "v_mov_b64 v[136:137], 0\n" \
    "v_mov_b64 v[138:139], 0\n" \
    "v_mov_b64 v[140:141], 0\n" \
    "v_mov_b64 v[142:143], 0\n" \
    "s_nop 1\n" \
    "v_smfmac_f32_32x32x32_bf16 v[128:143], v[44:47], v[8:15], v84\n" \
    "v_smfmac_f32_32x32x32_bf16 v[128:143], v[48:51], v[16:23], v84\n" \
    "v_smfmac_f32_32x32x32_bf16 v[128:143], v[52:55], v[24:31], v84\n" \
    "v_mov_b32 v34, v64\n" \
    "v_mov_b32 v35, v64\n" \
    "s_nop 1\n" \
    "v_permlane32_swap_b32 v34, v35\n" \
    "v_rcp_f32 v35, v35\n" \
    "s_nop 0\n" \
    "v_mul_f32 v56, v35, v56\n" \
    "v_mul_f32 v57, v35, v57\n" \
    "v_mul_f32 v58, v35, v58\n" \
    "v_mul_f32 v59, v35, v59\n" \
    "v_mul_f32 v60, v35, v60\n" \
    "v_mul_f32 v61, v35, v61\n" \
    "v_mul_f32 v62, v35, v62\n" \
    "v_mul_f32 v63, v35, v63\n" \
    "v_mul_f32 v64, v35, v64\n" \
    "v_mul_f32 v65, v35, v65\n" \
    "v_mul_f32 v66, v35, v66\n" \
    "v_mul_f32 v67, v35, v67\n" \
    "v_cvt_pk_bf16_f32 v56, v56, v57\n" \
    "v_cvt_pk_bf16_f32 v57, v58, v59\n" \
    "v_cvt_pk_bf16_f32 v58, v60, v61\n" \
    "v_cvt_pk_bf16_f32 v59, v62, v63\n" \
    "v_cvt_pk_bf16_f32 v60, v64, v65\n" \
    "v_cvt_pk_bf16_f32 v61, v66, v67\n" \
    "global_store_dwordx4 v95, v[56:59], s[58:59] offset:0 sc1\n" \
    "s_mov_b32 exec_hi, 0\n" \
    "global_store_dwordx2 v95, v[60:61], s[60:61] offset:0 sc1\n" \
    "s_mov_b32 exec_hi, -1\n" \
    "v_mov_b32 v34, v136\n" \
    "v_mov_b32 v35, v136\n" \
    "s_nop 1\n" \
    "v_permlane32_swap_b32 v34, v35\n" \
    "v_rcp_f32 v35, v35\n" \
    "s_nop 0\n" \
    "v_mul_f32 v128, v35, v128\n" \
    "v_mul_f32 v129, v35, v129\n" \
    "v_mul_f32 v130, v35, v130\n" \
    "v_mul_f32 v131, v35, v131\n" \
    "v_mul_f32 v132, v35, v132\n" \
    "v_mul_f32 v133, v35, v133\n" \
    "v_mul_f32 v134, v35, v134\n" \
    "v_mul_f32 v135, v35, v135\n" \
    "v_mul_f32 v136, v35, v136\n" \
    "v_mul_f32 v137, v35, v137\n" \
    "v_mul_f32 v138, v35, v138\n" \
    "v_mul_f32 v139, v35, v139\n" \
    "v_cvt_pk_bf16_f32 v128, v128, v129\n" \
    "v_cvt_pk_bf16_f32 v129, v130, v131\n" \
    "v_cvt_pk_bf16_f32 v130, v132, v133\n" \
    "v_cvt_pk_bf16_f32 v131, v134, v135\n" \
    "v_cvt_pk_bf16_f32 v132, v136, v137\n" \
    "v_cvt_pk_bf16_f32 v133, v138, v139\n" \
    "global_store_dwordx4 v95, v[128:131], s[58:59] offset:512 sc1\n" \
    "s_mov_b32 exec_hi, 0\n" \
    "global_store_dwordx2 v95, v[132:133], s[60:61] offset:512 sc1\n" \
    "s_mov_b32 exec_hi, -1\n" \
    ".Latt_b:\n" \
    ".Lis3: s_cbranch_execz .Lis_ret\n" \
    "s_mov_b32 s50, 113664\n" \
    "s_setprio 0\n" \
    "v_exp_f32 v96, v96\n" \
    "s_setprio 1\n" \
    "v_exp_f32 v97, v97\n" \
    "v_exp_f32 v98, v98\n" \
    "v_exp_f32 v99, v99\n" \
    "v_exp_f32 v100, v100\n" \
    "v_exp_f32 v101, v101\n" \
    "v_exp_f32 v102, v102\n" \
    "v_exp_f32 v103, v103\n" \
    "v_exp_f32 v104, v104\n" \
    "v_exp_f32 v105, v105\n" \
    "v_exp_f32 v106, v106\n" \
    "v_exp_f32 v107, v107\n" \
    "v_exp_f32 v108, v108\n" \
    "v_exp_f32 v109, v109\n" \
    "v_exp_f32 v110, v110\n" \
    "v_exp_f32 v111, v111\n" \
    "v_exp_f32 v112, v112\n" \
    "v_exp_f32 v113, v113\n" \
    "v_exp_f32 v114, v114\n" \
    "v_exp_f32 v115, v115\n" \
    "v_exp_f32 v116, v116\n" \
    "v_exp_f32 v117, v117\n" \
    "v_exp_f32 v118, v118\n" \
    "v_exp_f32 v119, v119\n" \
    "v_exp_f32 v120, v120\n" \
    "v_exp_f32 v121, v121\n" \
    "v_exp_f32 v122, v122\n" \
    "v_exp_f32 v123, v123\n" \
    "v_exp_f32 v124, v124\n" \
    "v_exp_f32 v125, v125\n" \
    "v_exp_f32 v126, v126\n" \
    "v_exp_f32 v127, v127\n" \
    "s_setprio 2\n" \
    "s_waitcnt lgkmcnt(0)\n" \
    "v_mfma_f32_32x32x4_2b_bf16 v[128:159], v[234:235], v[232:233], v[160:191] cbsz:1 abid:1\n" \
    "s_add_u32 s46, s46, 1\n" \
    "v_mov_b32 v95, s46\n" \
    "ds_write_b32 v93, v95\n" \
    "s_mov_b64 exec, s[48:49]\n" \
    "ds_read_b128 v[248:251], v86 offset:1280\n" \
    "ds_read_b128 v[252:255], v86 offset:1920\n" \
    "s_mov_b64 exec, -1\n" \
    "s_nop 1\n" \
    "v_smfmac_f32_32x32x32_bf16 v[192:207], v[240:243], v[96:103], v84\n" \
    "s_cmp_ge_u32 %[wid], 4\n" \
    "s_cselect_b32 s66, 88064, s50\n" \
    "s_and_b32 s51, %[wid], 3\n" \
    "v_smfmac_f32_32x32x32_bf16 v[192:207], v[244:247], v[104:111], v84\n" \
    "s_cmp_eq_u32 s51, 0\n" \
    "s_cselect_b32 s66, 4294946816, s66\n" \
    "s_sub_u32 s47, s47, s66\n" \
    "v_smfmac_f32_32x32x32_bf16 v[208:223], v[240:243], v[112:119], v84\n" \
    "v_mfma_f32_32x32x16_bf16 v[128:143], v[236:239], v[224:227], v[128:143]\n" \
    "s_cmp_eq_u32 s51, 3\n" \
    "s_cselect_b32 s66, 768000, 0\n" \
    "s_add_u32 s66, s66, %[wa]\n" \
    "s_add_u32 s66, s66, 35840\n" \
    "v_mfma_f32_32x32x16_bf16 v[144:159], v[236:239], v[228:231], v[144:159]\n" \
    "ds_read_b64 v[236:237], v85 offset:5120\n" \
    "ds_read_b64 v[238:239], v85 offset:5632\n" \
    "ds_read_b64 v[234:235], v85 offset:6144\n" \
    "s_add_u32 s58, s40, s66\n" \
    "s_addc_u32 s59, s41, 0\n" \
    "v_smfmac_f32_32x32x32_bf16 v[208:223], v[244:247], v[120:127], v84\n" \
    "s_setprio 0\n" \
    "v_exp_f32 v128, v128\n" \
    "s_setprio 1\n" \
    "v_exp_f32 v129, v129\n" \
    "v_exp_f32 v130, v130\n" \
    "v_exp_f32 v131, v131\n" \
    "v_exp_f32 v132, v132\n" \
    "v_exp_f32 v133, v133\n" \
    "v_exp_f32 v134, v134\n" \
    "v_exp_f32 v135, v135\n" \
    "v_exp_f32 v136, v136\n" \
    "v_exp_f32 v137, v137\n" \
    "v_exp_f32 v138, v138\n" \
    "v_exp_f32 v139, v139\n" \
    "v_exp_f32 v140, v140\n" \
    "v_exp_f32 v141, v141\n" \
    "v_exp_f32 v142, v142\n" \
    "v_exp_f32 v143, v143\n" \
    "v_exp_f32 v144, v144\n" \
    "v_exp_f32 v145, v145\n" \
    "v_exp_f32 v146, v146\n" \
    "v_exp_f32 v147, v147\n" \
    "v_exp_f32 v148, v148\n" \
    "v_exp_f32 v149, v149\n" \
    "v_exp_f32 v150, v150\n" \
    "v_exp_f32 v151, v151\n" \
    "v_exp_f32 v152, v152\n" \
    "v_exp_f32 v153, v153\n" \
    "v_exp_f32 v154, v154\n" \
    "v_exp_f32 v155, v155\n" \
    "v_exp_f32 v156, v156\n" \
    "v_exp_f32 v157, v157\n" \
    "v_exp_f32 v158, v158\n" \
    "v_exp_f32 v159, v159\n" \
    "s_setprio 2\n" \
    "s_waitcnt lgkmcnt(0)\n" \
    "v_mfma_f32_32x32x4_2b_bf16 v[96:127], v[234:235], v[232:233], v[160:191] cbsz:1 abid:0\n" \
    "ds_read_b32 v95, v94\n" \
    "s_mov_b64 exec, s[48:49]\n" \
    "ds_read_b128 v[240:243], v86 offset:5120\n" \
    "ds_read_b128 v[244:247], v86 offset:5760\n" \
    "s_mov_b64 exec, -1\n" \
    "s_nop 3\n" \
    "v_smfmac_f32_32x32x32_bf16 v[192:207], v[248:251], v[128:135], v84\n" \
    "s_waitcnt lgkmcnt(2)\n" \
    "v_cmp_gt_u32 vcc, s46, v95\n" \
    "s_movk_i32 s50, 0x4000\n" \
    "s_cbranch_vccnz .Latt_spin17\n" \
    ".Latt_go17:\n" \
    "v_smfmac_f32_32x32x32_bf16 v[192:207], v[252:255], v[136:143], v84\n" \
    "s_add_u32 m0, s44, 0\n" \
    "s_nop 0\n" \
    "global_load_lds_dwordx4 v87, s[42:43] sc1\n" \
    "v_smfmac_f32_32x32x32_bf16 v[208:223], v[248:251], v[144:151], v84\n" \
    "s_add_u32 m0, s44, 1024\n" \
    "s_nop 0\n" \
    "global_load_lds_dwordx4 v88, s[42:43] sc1\n" \
    "v_mfma_f32_32x32x16_bf16 v[96:111], v[236:239], v[224:227], v[96:111]\n" \
    "s_add_u32 m0, s44, 2048\n" \
    "s_nop 0\n" \
    "global_load_lds_dwordx4 v89, s[42:43] sc1\n" \
    "s_nop 0\n" \
    "v_mfma_f32_32x32x16_bf16 v[112:127], v[236:239], v[228:231], v[112:127]\n" \
    "ds_read_b64 v[236:237], v85 offset:5376\n" \
    "ds_read_b64 v[238:239], v85 offset:5888\n" \
    "s_add_u32 m0, s44, 3072\n" \
    "s_nop 0\n" \
    "global_load_lds_dwordx4 v90, s[42:43] sc1\n" \
    "s_add_u32 m0, s44, 4096\n" \
    "s_nop 0\n" \
    "global_load_lds_dwordx4 v91, s[42:43] sc1\n" \
    "s_add_u32 s42, s42, 327680\n" \
    "s_addc_u32 s43, s43, 0\n" \
    "v_smfmac_f32_32x32x32_bf16 v[208:223], v[252:255], v[152:159], v84\n" \
    "s_setprio 0\n" \
    "v_exp_f32 v96, v96\n" \
    "s_setprio 1\n" \
    "v_exp_f32 v97, v97\n" \
    "v_exp_f32 v98, v98\n" \
    "v_exp_f32 v99, v99\n" \
    "v_exp_f32 v100, v100\n" \
    "v_exp_f32 v101, v101\n" \
    "v_exp_f32 v102, v102\n" \
    "v_exp_f32 v103, v103\n" \
    "v_exp_f32 v104, v104\n" \
    "v_exp_f32 v105, v105\n" \
    "v_exp_f32 v106, v106\n" \
    "v_exp_f32 v107, v107\n" \
    "v_exp_f32 v108, v108\n" \
    "v_exp_f32 v109, v109\n" \
    "v_exp_f32 v110, v110\n" \
    "v_exp_f32 v111, v111\n" \
    "v_exp_f32 v112, v112\n" \
    "v_exp_f32 v113, v113\n" \
    "v_exp_f32 v114, v114\n" \
    "v_exp_f32 v115, v115\n" \
    "v_exp_f32 v116, v116\n" \
    "v_exp_f32 v117, v117\n" \
    "v_exp_f32 v118, v118\n" \
    "v_exp_f32 v119, v119\n" \
    "v_exp_f32 v120, v120\n" \
    "v_exp_f32 v121, v121\n" \
    "v_exp_f32 v122, v122\n" \
    "v_exp_f32 v123, v123\n" \
    "v_exp_f32 v124, v124\n" \
    "v_exp_f32 v125, v125\n" \
    "v_exp_f32 v126, v126\n" \
    "v_exp_f32 v127, v127\n" \
    "s_setprio 2\n" \
    "s_waitcnt lgkmcnt(0)\n" \
    "v_mfma_f32_32x32x4_2b_bf16 v[128:159], v[234:235], v[232:233], v[160:191] cbsz:1 abid:1\n" \
    "s_mov_b64 exec, s[48:49]\n" \
    "ds_read_b128 v[248:251], v86 offset:6400\n" \
    "ds_read_b128 v[252:255], v86 offset:7040\n" \
    "s_mov_b64 exec, -1\n" \
    "s_nop 4\n" \
    "v_smfmac_f32_32x32x32_bf16 v[192:207], v[240:243], v[96:103], v84\n" \
    "s_and_b32 s66, %[b], 7\n" \
    "s_lshl_b32 s66, s66, 2\n" \
    "s_lshr_b32 s67, %[b], 6\n" \
    "s_add_u32 s66, s66, s67\n" \
    "v_smfmac_f32_32x32x32_bf16 v[192:207], v[244:247], v[104:111], v84\n" \
    "s_lshl_b32 s66, s66, 1\n" \
    "s_lshr_b32 s67, %[wid], 2\n" \
    "s_add_u32 s66, s66, s67\n" \
    "s_mul_i32 s66, s66, 40960\n" \
    "v_smfmac_f32_32x32x32_bf16 v[208:223], v[240:243], v[112:119], v84\n" \
    "v_mfma_f32_32x32x16_bf16 v[128:143], v[236:239], v[224:227], v[128:143]\n" \
    "s_add_u32 s66, s66, 15728640\n" \
    "s_add_u32 s60, s40, s66\n" \
    "s_addc_u32 s61, s41, 0\n" \
    "s_nop 0\n" \
    "v_mfma_f32_32x32x16_bf16 v[144:159], v[236:239], v[228:231], v[144:159]\n" \
    "ds_read_b64 v[236:237], v85 offset:10240\n" \
    "ds_read_b64 v[238:239], v85 offset:10752\n" \
    "ds_read_b64 v[234:235], v85 offset:11264\n" \
    "s_add_u32 s56, s56, 15360\n" \
    "s_addc_u32 s57, s57, 0\n" \
    "v_smfmac_f32_32x32x32_bf16 v[208:223], v[244:247], v[120:127], v84\n" \
    "s_setprio 0\n" \
    "v_exp_f32 v128, v128\n" \
    "s_setprio 1\n" \
    "v_exp_f32 v129, v129\n" \
    "v_exp_f32 v130, v130\n" \
    "v_exp_f32 v131, v131\n" \
    "v_exp_f32 v132, v132\n" \
    "v_exp_f32 v133, v133\n" \
    "v_exp_f32 v134, v134\n" \
    "v_exp_f32 v135, v135\n" \
    "v_exp_f32 v136, v136\n" \
    "v_exp_f32 v137, v137\n" \
    "v_exp_f32 v138, v138\n" \
    "v_exp_f32 v139, v139\n" \
    "v_exp_f32 v140, v140\n" \
    "v_exp_f32 v141, v141\n" \
    "v_exp_f32 v142, v142\n" \
    "v_exp_f32 v143, v143\n" \
    "v_exp_f32 v144, v144\n" \
    "v_exp_f32 v145, v145\n" \
    "v_exp_f32 v146, v146\n" \
    "v_exp_f32 v147, v147\n" \
    "v_exp_f32 v148, v148\n" \
    "v_exp_f32 v149, v149\n" \
    "v_exp_f32 v150, v150\n" \
    "v_exp_f32 v151, v151\n" \
    "v_exp_f32 v152, v152\n" \
    "v_exp_f32 v153, v153\n" \
    "v_exp_f32 v154, v154\n" \
    "v_exp_f32 v155, v155\n" \
    "v_exp_f32 v156, v156\n" \
    "v_exp_f32 v157, v157\n" \
    "v_exp_f32 v158, v158\n" \
    "v_exp_f32 v159, v159\n" \
    "s_setprio 2\n" \
    "s_waitcnt lgkmcnt(0)\n" \
    "v_mfma_f32_32x32x4_2b_bf16 v[96:127], v[234:235], v[232:233], v[160:191] cbsz:1 abid:0\n" \
    "s_add_u32 s46, s46, 1\n" \
    "s_waitcnt vmcnt(0)\n" \
    "v_mov_b32 v95, s46\n" \
    "ds_write_b32 v93, v95\n" \
    "s_mov_b64 exec, s[48:49]\n" \
    "ds_read_b128 v[240:243], v86 offset:10240\n" \
    "ds_read_b128 v[244:247], v86 offset:10880\n" \
    "s_mov_b64 exec, -1\n" \
    "s_nop 0\n" \
    "v_smfmac_f32_32x32x32_bf16 v[192:207], v[248:251], v[128:135], v84\n" \
    "s_lshr_b32 s66, %[b], 5\n" \
    "s_lshl_b32 s66, s66, 5\n" \
    "s_add_u32 s66, s66, %[head]\n" \
    "s_lshl_b32 s66, s66, 7\n" \
    "v_smfmac_f32_32x32x32_bf16 v[192:207], v[252:255], v[136:143], v84\n" \
    "s_add_u32 s66, s66, 23003136\n" \
    "s_add_u32 s54, s40, s66\n" \
    "s_addc_u32 s55, s41, 0\n" \
    "v_smfmac_f32_32x32x32_bf16 v[208:223], v[248:251], v[144:151], v84\n" \
    "s_cmp_eq_u32 %[wid], 0\n" \
    "s_cselect_b64 s[52:53], 1, 0\n" \
    "v_mfma_f32_32x32x16_bf16 v[96:111], v[236:239], v[224:227], v[96:111]\n" \
    "s_bfe_u32 s66, %[b], 0x30003\n" \
    "s_mul_i32 s66, s66, 100\n" \
    "s_lshr_b32 s67, %[b], 6\n" \
    "s_add_u32 s66, s66, s67\n" \
    "v_mfma_f32_32x32x16_bf16 v[112:127], v[236:239], v[228:231], v[112:127]\n" \
    "ds_read_b64 v[236:237], v85 offset:10496\n" \
    "ds_read_b64 v[238:239], v85 offset:11008\n" \
    "s_mul_i32 s67, %[wid], 40\n" \
    "s_min_u32 s67, s67, 60\n" \
    "s_add_u32 s66, s66, s67\n" \
    "v_smfmac_f32_32x32x32_bf16 v[208:223], v[252:255], v[152:159], v84\n" \
    "s_setprio 0\n" \
    "v_exp_f32 v96, v96\n" \
    "s_setprio 1\n" \
    "v_exp_f32 v97, v97\n" \
    "v_exp_f32 v98, v98\n" \
    "v_exp_f32 v99, v99\n" \
    "v_exp_f32 v100, v100\n" \
    "v_exp_f32 v101, v101\n" \
    "v_exp_f32 v102, v102\n" \
    "v_exp_f32 v103, v103\n" \
    "v_exp_f32 v104, v104\n" \
    "v_exp_f32 v105, v105\n" \
    "v_exp_f32 v106, v106\n" \
    "v_exp_f32 v107, v107\n" \
    "v_exp_f32 v108, v108\n" \
    "v_exp_f32 v109, v109\n" \
    "v_exp_f32 v110, v110\n" \
    "v_exp_f32 v111, v111\n" \
    "v_exp_f32 v112, v112\n" \
    "v_exp_f32 v113, v113\n" \
    "v_exp_f32 v114, v114\n" \
    "v_exp_f32 v115, v115\n" \
    "v_exp_f32 v116, v116\n" \
    "v_exp_f32 v117, v117\n" \
    "v_exp_f32 v118, v118\n" \
    "v_exp_f32 v119, v119\n" \
    "v_exp_f32 v120, v120\n" \
    "v_exp_f32 v121, v121\n" \
    "v_exp_f32 v122, v122\n" \
    "v_exp_f32 v123, v123\n" \
    "v_exp_f32 v124, v124\n" \
    "v_exp_f32 v125, v125\n" \
    "v_exp_f32 v126, v126\n" \
    "v_exp_f32 v127, v127\n" \
    "s_setprio 2\n" \
    "s_waitcnt lgkmcnt(0)\n" \
    "v_mfma_f32_32x32x4_2b_bf16 v[128:159], v[234:235], v[232:233], v[160:191] cbsz:1 abid:1\n" \
    "ds_read_b32 v95, v94\n" \
    "s_mov_b64 exec, s[48:49]\n" \
    "ds_read_b128 v[248:251], v86 offset:11520\n" \
    "ds_read_b128 v[252:255], v86 offset:12160\n" \
    "s_mov_b64 exec, -1\n" \
    "s_nop 3\n" \
    "v_smfmac_f32_32x32x32_bf16 v[192:207], v[240:243], v[96:103], v84\n" \
    "s_waitcnt lgkmcnt(2)\n" \
    "v_cmp_gt_u32 vcc, s46, v95\n" \
    "s_movk_i32 s50, 0x4000\n" \
    "s_cbranch_vccnz .Latt_spin20\n" \
    ".Latt_go20:\n" \
    "v_smfmac_f32_32x32x32_bf16 v[192:207], v[244:247], v[104:111], v84\n" \
    "s_mov_b64 exec, s[52:53]\n" \
    "v_mov_b64 v[34:35], %[tag]\n" \
    "global_store_dwordx2 v92, v[34:35], s[54:55] sc1\n" \
    "s_mov_b64 exec, -1\n" \
    "v_smfmac_f32_32x32x32_bf16 v[208:223], v[240:243], v[112:119], v84\n" \
    "s_lshl_b32 s66, s66, 10\n" \
    "s_add_u32 s66, s66, 21790720\n" \
    "v_mfma_f32_32x32x16_bf16 v[128:143], v[236:239], v[224:227], v[128:143]\n" \
    "s_add_u32 s54, s40, s66\n" \
    "s_addc_u32 s55, s41, 0\n" \
    "s_cmp_lt_u32 s51, 3\n" \
    "s_cselect_b64 s[52:53], -1, 0\n" \
    "v_mfma_f32_32x32x16_bf16 v[144:159], v[236:239], v[228:231], v[144:159]\n" \
    "ds_read_b64 v[236:237], v85 offset:15360\n" \
    "ds_read_b64 v[238:239], v85 offset:15872\n" \
    "ds_read_b64 v[234:235], v85 offset:16384\n" \
    "s_nop 1\n" \
    "v_smfmac_f32_32x32x32_bf16 v[208:223], v[244:247], v[120:127], v84\n" \
    "s_setprio 0\n" \
    "v_exp_f32 v128, v128\n" \
    "s_setprio 1\n" \
    "v_exp_f32 v129, v129\n" \
    "v_exp_f32 v130, v130\n" \
    "v_exp_f32 v131, v131\n" \
    "v_exp_f32 v132, v132\n" \
    "v_exp_f32 v133, v133\n" \
    "v_exp_f32 v134, v134\n" \
    "v_exp_f32 v135, v135\n" \
    "v_exp_f32 v136, v136\n" \
    "v_exp_f32 v137, v137\n" \
    "v_exp_f32 v138, v138\n" \
    "v_exp_f32 v139, v139\n" \
    "v_exp_f32 v140, v140\n" \
    "v_exp_f32 v141, v141\n" \
    "v_exp_f32 v142, v142\n" \
    "v_exp_f32 v143, v143\n" \
    "v_exp_f32 v144, v144\n" \
    "v_exp_f32 v145, v145\n" \
    "v_exp_f32 v146, v146\n" \
    "v_exp_f32 v147, v147\n" \
    "v_exp_f32 v148, v148\n" \
    "v_exp_f32 v149, v149\n" \
    "v_exp_f32 v150, v150\n" \
    "v_exp_f32 v151, v151\n" \
    "v_exp_f32 v152, v152\n" \
    "v_exp_f32 v153, v153\n" \
    "v_exp_f32 v154, v154\n" \
    "v_exp_f32 v155, v155\n" \
    "v_exp_f32 v156, v156\n" \
    "v_exp_f32 v157, v157\n" \
    "v_exp_f32 v158, v158\n" \
    "v_exp_f32 v159, v159\n" \
    "s_setprio 2\n" \
    "s_waitcnt lgkmcnt(0)\n" \
    "v_mfma_f32_32x32x4_2b_bf16 v[96:127], v[234:235], v[232:233], v[160:191] cbsz:1 abid:0\n" \
    "s_mov_b64 exec, s[48:49]\n" \
    "ds_read_b128 v[240:243], v86 offset:15360\n" \
    "ds_read_b128 v[244:247], v86 offset:16000\n" \
    "s_mov_b64 exec, -1\n" \
    "s_nop 4\n" \
    "v_smfmac_f32_32x32x32_bf16 v[192:207], v[248:251], v[128:135], v84\n" \
    "s_add_u32 m0, s47, 0\n" \
    "s_mov_b64 exec, s[52:53]\n" \
    "global_load_lds_dwordx4 v92, s[56:57] sc1\n" \
    "s_mov_b64 exec, -1\n" \
    "s_add_u32 s56, s56, 1024\n" \
    "s_addc_u32 s57, s57, 0\n" \
    "v_smfmac_f32_32x32x32_bf16 v[192:207], v[252:255], v[136:143], v84\n" \
    "s_add_u32 m0, s47, 1024\n" \
    "s_mov_b64 exec, s[52:53]\n" \
    "global_load_lds_dwordx4 v92, s[56:57] sc1\n" \
    "s_mov_b64 exec, -1\n" \
    "s_add_u32 s56, s56, 1024\n" \
    "s_addc_u32 s57, s57, 0\n" \
    "v_smfmac_f32_32x32x32_bf16 v[208:223], v[248:251], v[144:151], v84\n" \
    "s_add_u32 m0, s47, 2048\n" \
    "s_mov_b64 exec, s[52:53]\n" \
    "global_load_lds_dwordx4 v92, s[56:57] sc1\n" \
    "s_mov_b64 exec, -1\n" \
    "s_add_u32 s56, s56, 1024\n" \
    "s_addc_u32 s57, s57, 0\n" \
    "v_mfma_f32_32x32x16_bf16 v[96:111], v[236:239], v[224:227], v[96:111]\n" \
    "s_add_u32 m0, s47, 3072\n" \
    "s_mov_b64 exec, s[52:53]\n" \
    "global_load_lds_dwordx4 v92, s[56:57] sc1\n" \
    "s_mov_b64 exec, -1\n" \
    "s_add_u32 s56, s56, 1024\n" \
    "s_addc_u32 s57, s57, 0\n" \
    "v_mfma_f32_32x32x16_bf16 v[112:127], v[236:239], v[228:231], v[112:127]\n" \
    "ds_read_b64 v[236:237], v85 offset:15616\n" \
    "ds_read_b64 v[238:239], v85 offset:16128\n" \
    "s_add_u32 m0, s47, 4096\n" \
    "s_mov_b64 exec, s[52:53]\n" \
    "global_load_lds_dwordx4 v92, s[56:57] sc1\n" \
    "s_mov_b64 exec, -1\n" \
    "s_add_u32 s56, s56, 1024\n" \
    "s_addc_u32 s57, s57, 0\n" \
    "s_cmp_lt_u32 %[wid], 3\n" \
    "s_cselect_b64 s[52:53], -1, 0\n" \
    "v_smfmac_f32_32x32x32_bf16 v[208:223], v[252:255], v[152:159], v84\n" \
    "s_setprio 0\n" \
    "v_exp_f32 v96, v96\n" \
    "s_setprio 1\n" \
    "v_exp_f32 v97, v97\n" \
    "v_exp_f32 v98, v98\n" \
    "v_exp_f32 v99, v99\n" \
    "v_exp_f32 v100, v100\n" \
    "v_exp_f32 v101, v101\n" \
    "v_exp_f32 v102, v102\n" \
    "v_exp_f32 v103, v103\n" \
    "v_exp_f32 v104, v104\n" \
    "v_exp_f32 v105, v105\n" \
    "v_exp_f32 v106, v106\n" \
    "v_exp_f32 v107, v107\n" \
    "v_exp_f32 v108, v108\n" \
    "v_exp_f32 v109, v109\n" \
    "v_exp_f32 v110, v110\n" \
    "v_exp_f32 v111, v111\n" \
    "v_exp_f32 v112, v112\n" \
    "v_exp_f32 v113, v113\n" \
    "v_exp_f32 v114, v114\n" \
    "v_exp_f32 v115, v115\n" \
    "v_exp_f32 v116, v116\n" \
    "v_exp_f32 v117, v117\n" \
    "v_exp_f32 v118, v118\n" \
    "v_exp_f32 v119, v119\n" \
    "v_exp_f32 v120, v120\n" \
    "v_exp_f32 v121, v121\n" \
    "v_exp_f32 v122, v122\n" \
    "v_exp_f32 v123, v123\n" \
    "v_exp_f32 v124, v124\n" \
    "v_exp_f32 v125, v125\n" \
    "v_exp_f32 v126, v126\n" \
    "v_exp_f32 v127, v127\n" \
    "s_setprio 2\n" \
    "s_waitcnt lgkmcnt(0)\n" \
    "v_mfma_f32_32x32x4_2b_bf16 v[128:159], v[234:235], v[232:233], v[160:191] cbsz:1 abid:1\n" \
    "v_add_u32 v85, 0xffff2000, v85\n" \
    "s_mov_b64 exec, s[48:49]\n" \
    "ds_read_b128 v[248:251], v86 offset:16640\n" \
    "ds_read_b128 v[252:255], v86 offset:17280\n" \
    "s_mov_b64 exec, -1\n" \
    "s_nop 3\n" \
    "v_smfmac_f32_32x32x32_bf16 v[192:207], v[240:243], v[96:103], v84\n" \
    "s_mov_b64 exec, s[52:53]\n" \
    "global_load_dwordx4 v[44:47], v92, s[54:55] sc1\n" \
    "s_mov_b64 exec, -1\n" \
    "s_add_u32 s54, s54, 4096\n" \
    "s_addc_u32 s55, s55, 0\n" \
    "v_smfmac_f32_32x32x32_bf16 v[192:207], v[244:247], v[104:111], v84\n" \
    "s_mov_b64 exec, s[52:53]\n" \
    "global_load_dwordx4 v[48:51], v92, s[54:55] sc1\n" \
    "s_mov_b64 exec, -1\n" \
    "s_add_u32 s54, s54, 4096\n" \
    "s_addc_u32 s55, s55, 0\n" \
    "v_smfmac_f32_32x32x32_bf16 v[208:223], v[240:243], v[112:119], v84\n" \
    "s_mov_b64 exec, s[52:53]\n" \
    "global_load_dwordx4 v[52:55], v92, s[54:55] sc1\n" \
    "s_mov_b64 exec, -1\n" \
    "s_add_u32 s54, s54, 4096\n" \
    "s_addc_u32 s55, s55, 0\n" \
    "v_mfma_f32_32x32x16_bf16 v[128:143], v[236:239], v[224:227], v[128:143]\n" \
    "s_mov_b64 exec, s[52:53]\n" \
    "global_load_dwordx4 v[56:59], v92, s[54:55] sc1\n" \
    "s_mov_b64 exec, -1\n" \
    "s_add_u32 s54, s54, 4096\n" \
    "s_addc_u32 s55, s55, 0\n" \
    "v_mfma_f32_32x32x16_bf16 v[144:159], v[236:239], v[228:231], v[144:159]\n" \
    "ds_read_b64 v[236:237], v85 offset:0\n" \
    "ds_read_b64 v[238:239], v85 offset:512\n" \
    "ds_read_b64 v[234:235], v85 offset:1024\n" \
    "s_mov_b64 exec, s[52:53]\n" \
    "global_load_dwordx4 v[60:63], v92, s[54:55] sc1\n" \
    "s_mov_b64 exec, -1\n" \
    "s_add_u32 s54, s54, 4096\n" \
    "s_addc_u32 s55, s55, 0\n" \
    "v_smfmac_f32_32x32x32_bf16 v[208:223], v[244:247], v[120:127], v84\n" \
    "s_setprio 0\n" \
    "v_exp_f32 v128, v128\n" \
    "s_setprio 1\n" \
    "v_exp_f32 v129, v129\n" \
    "v_exp_f32 v130, v130\n" \
    "v_exp_f32 v131, v131\n" \
    "v_exp_f32 v132, v132\n" \
    "v_exp_f32 v133, v133\n" \
    "v_exp_f32 v134, v134\n" \
    "v_exp_f32 v135, v135\n" \
    "v_exp_f32 v136, v136\n" \
    "v_exp_f32 v137, v137\n" \
    "v_exp_f32 v138, v138\n" \
    "v_exp_f32 v139, v139\n" \
    "v_exp_f32 v140, v140\n" \
    "v_exp_f32 v141, v141\n" \
    "v_exp_f32 v142, v142\n" \
    "v_exp_f32 v143, v143\n" \
    "v_exp_f32 v144, v144\n" \
    "v_exp_f32 v145, v145\n" \
    "v_exp_f32 v146, v146\n" \
    "v_exp_f32 v147, v147\n" \
    "v_exp_f32 v148, v148\n" \
    "v_exp_f32 v149, v149\n" \
    "v_exp_f32 v150, v150\n" \
    "v_exp_f32 v151, v151\n" \
    "v_exp_f32 v152, v152\n" \
    "v_exp_f32 v153, v153\n" \
    "v_exp_f32 v154, v154\n" \
    "v_exp_f32 v155, v155\n" \
    "v_exp_f32 v156, v156\n" \
    "v_exp_f32 v157, v157\n" \
    "v_exp_f32 v158, v158\n" \
    "v_exp_f32 v159, v159\n" \
    "s_setprio 2\n" \
    "s_waitcnt lgkmcnt(0)\n" \
    "v_mfma_f32_32x32x4_2b_bf16 v[96:127], v[234:235], v[232:233], v[160:191] cbsz:1 abid:0\n" \
    "v_add_u32 v86, 0xffff2000, v86\n" \
    "s_mov_b64 exec, s[48:49]\n" \
    "ds_read_b128 v[240:243], v86 offset:0\n" \
    "ds_read_b128 v[244:247], v86 offset:640\n" \
    "s_mov_b64 exec, -1\n" \
    "s_nop 3\n" \
    "v_smfmac_f32_32x32x32_bf16 v[192:207], v[248:251], v[128:135], v84\n" \
    "s_mov_b64 exec, s[52:53]\n" \
    "global_load_dwordx4 v[64:67], v92, s[54:55] sc1\n" \
    "s_mov_b64 exec, -1\n" \
    "s_add_u32 s54, s54, 4096\n" \
    "s_addc_u32 s55, s55, 0\n" \
    "v_smfmac_f32_32x32x32_bf16 v[192:207], v[252:255], v[136:143], v84\n" \
    "s_mov_b64 exec, s[52:53]\n" \
    "global_load_dwordx4 v[68:71], v92, s[54:55] sc1\n" \
    "s_mov_b64 exec, -1\n" \
    "s_add_u32 s54, s54, 4096\n" \
    "s_addc_u32 s55, s55, 0\n" \
    "v_smfmac_f32_32x32x32_bf16 v[208:223], v[248:251], v[144:151], v84\n" \
    "s_mov_b64 exec, s[52:53]\n" \
    "global_load_dwordx4 v[72:75], v92, s[54:55] sc1\n" \
    "s_mov_b64 exec, -1\n" \
    "s_add_u32 s54, s54, 4096\n" \
    "s_addc_u32 s55, s55, 0\n" \
    "v_mfma_f32_32x32x16_bf16 v[96:111], v[236:239], v[224:227], v[96:111]\n" \
    "s_mov_b64 exec, s[52:53]\n" \
    "global_load_dwordx4 v[76:79], v92, s[54:55] sc1\n" \
    "s_mov_b64 exec, -1\n" \
    "s_add_u32 s54, s54, 4096\n" \
    "s_addc_u32 s55, s55, 0\n" \
    "v_mfma_f32_32x32x16_bf16 v[112:127], v[236:239], v[228:231], v[112:127]\n" \
    "ds_read_b64 v[236:237], v85 offset:256\n" \
    "ds_read_b64 v[238:239], v85 offset:768\n" \
    "s_mov_b64 exec, s[52:53]\n" \
    "global_load_dwordx4 v[80:83], v92, s[54:55] sc1\n" \
    "s_mov_b64 exec, -1\n" \
    "s_add_u32 s54, s54, 4096\n" \
    "s_addc_u32 s55, s55, 0\n" \
    "v_smfmac_f32_32x32x32_bf16 v[208:223], v[252:255], v[152:159], v84\n" \
    "s_setprio 0\n" \
    "v_exp_f32 v96, v96\n" \
    "s_setprio 1\n" \
    "v_exp_f32 v97, v97\n" \
    "v_exp_f32 v98, v98\n" \
    "v_exp_f32 v99, v99\n" \
    "v_exp_f32 v100, v100\n" \
    "v_exp_f32 v101, v101\n" \
    "v_exp_f32 v102, v102\n" \
    "v_exp_f32 v103, v103\n" \
    "v_exp_f32 v104, v104\n" \
    "v_exp_f32 v105, v105\n" \
    "v_exp_f32 v106, v106\n" \
    "v_exp_f32 v107, v107\n" \
    "v_exp_f32 v108, v108\n" \
    "v_exp_f32 v109, v109\n" \
    "v_exp_f32 v110, v110\n" \
    "v_exp_f32 v111, v111\n" \
    "v_exp_f32 v112, v112\n" \
    "v_exp_f32 v113, v113\n" \
    "v_exp_f32 v114, v114\n" \
    "v_exp_f32 v115, v115\n" \
    "v_exp_f32 v116, v116\n" \
    "v_exp_f32 v117, v117\n" \
    "v_exp_f32 v118, v118\n" \
    "v_exp_f32 v119, v119\n" \
    "v_exp_f32 v120, v120\n" \
    "v_exp_f32 v121, v121\n" \
    "v_exp_f32 v122, v122\n" \
    "v_exp_f32 v123, v123\n" \
    "v_exp_f32 v124, v124\n" \
    "v_exp_f32 v125, v125\n" \
    "v_exp_f32 v126, v126\n" \
    "v_exp_f32 v127, v127\n" \
    "s_setprio 2\n" \
    "s_waitcnt lgkmcnt(0)\n" \
    "v_mfma_f32_32x32x4_2b_bf16 v[128:159], v[234:235], v[232:233], v[160:191] cbsz:1 abid:1\n" \
    "s_add_u32 s46, s46, 1\n" \
    "v_mov_b32 v95, s46\n" \
    "ds_write_b32 v93, v95\n" \
    "s_mov_b64 exec, s[48:49]\n" \
    "ds_read_b128 v[248:251], v86 offset:1280\n" \
    "ds_read_b128 v[252:255], v86 offset:1920\n" \
    "s_mov_b64 exec, -1\n" \
    "s_nop 1\n" \
    "v_smfmac_f32_32x32x32_bf16 v[192:207], v[240:243], v[96:103], v84\n" \
    "s_and_b32 s66, %[b], 7\n" \
    "s_lshl_b32 s66, s66, 1\n" \
    "s_lshr_b32 s67, %[wid], 2\n" \
    "s_add_u32 s66, s66, s67\n" \
    "v_smfmac_f32_32x32x32_bf16 v[192:207], v[244:247], v[104:111], v84\n" \
    "s_lshl_b32 s66, s66, 11\n" \
    "s_add_u32 s66, s66, 23003136\n" \
    "s_lshl_b32 s67, s51, 8\n" \
    "v_smfmac_f32_32x32x32_bf16 v[208:223], v[240:243], v[112:119], v84\n" \
    "s_lshl_b32 s62, %[lp], 5\n" \
    "s_add_u32 s62, s62, %[head]\n" \
    "s_lshl_b32 s62, s62, 1\n" \
    "s_lshr_b32 s63, %[wid], 2\n" \
    "s_add_u32 s62, s62, s63\n" \
    "v_mfma_f32_32x32x16_bf16 v[128:143], v[236:239], v[224:227], v[128:143]\n" \
    "v_and_b32 v6, 0xff, v92\n" \
    "v_lshl_add_u32 v5, v6, 3, s66\n" \
    "v_and_b32 v7, 0x300, v92\n" \
    "s_nop 0\n" \
    "v_mfma_f32_32x32x16_bf16 v[144:159], v[236:239], v[228:231], v[144:159]\n" \
    "ds_read_b64 v[236:237], v85 offset:5120\n" \
    "ds_read_b64 v[238:239], v85 offset:5632\n" \
    "ds_read_b64 v[234:235], v85 offset:6144\n" \
    "v_lshl_add_u32 v6, v7, 2, v6\n" \
    "v_add_u32 v6, s67, v6\n" \
    "v_smfmac_f32_32x32x32_bf16 v[208:223], v[244:247], v[120:127], v84\n" \
    "s_setprio 0\n" \
    "v_exp_f32 v128, v128\n" \
    "s_setprio 1\n" \
    "v_exp_f32 v129, v129\n" \
    "v_exp_f32 v130, v130\n" \
    "v_exp_f32 v131, v131\n" \
    "v_exp_f32 v132, v132\n" \
    "v_exp_f32 v133, v133\n" \
    "v_exp_f32 v134, v134\n" \
    "v_exp_f32 v135, v135\n" \
    "v_exp_f32 v136, v136\n" \
    "v_exp_f32 v137, v137\n" \
    "v_exp_f32 v138, v138\n" \
    "v_exp_f32 v139, v139\n" \
    "v_exp_f32 v140, v140\n" \
    "v_exp_f32 v141, v141\n" \
    "v_exp_f32 v142, v142\n" \
    "v_exp_f32 v143, v143\n" \
    "v_exp_f32 v144, v144\n" \
    "v_exp_f32 v145, v145\n" \
    "v_exp_f32 v146, v146\n" \
    "v_exp_f32 v147, v147\n" \
    "v_exp_f32 v148, v148\n" \
    "v_exp_f32 v149, v149\n" \
    "v_exp_f32 v150, v150\n" \
    "v_exp_f32 v151, v151\n" \
    "v_exp_f32 v152, v152\n" \
    "v_exp_f32 v153, v153\n" \
    "v_exp_f32 v154, v154\n" \
    "v_exp_f32 v155, v155\n" \
    "v_exp_f32 v156, v156\n" \
    "v_exp_f32 v157, v157\n" \
    "v_exp_f32 v158, v158\n" \
    "v_exp_f32 v159, v159\n" \
    "s_setprio 2\n" \
    "s_waitcnt lgkmcnt(0)\n" \
    "v_mfma_f32_32x32x4_2b_bf16 v[96:127], v[234:235], v[232:233], v[160:191] cbsz:1 abid:0\n" \
    "ds_read_b32 v95, v94\n" \
    "s_mov_b64 exec, s[48:49]\n" \
    "ds_read_b128 v[240:243], v86 offset:5120\n" \
    "ds_read_b128 v[244:247], v86 offset:5760\n" \
    "s_mov_b64 exec, -1\n" \
    "s_nop 3\n" \
    "v_smfmac_f32_32x32x32_bf16 v[192:207], v[248:251], v[128:135], v84\n" \
    "s_waitcnt lgkmcnt(2)\n" \
    "v_cmp_gt_u32 vcc, s46, v95\n" \
    "s_movk_i32 s50, 0x4000\n" \
    "s_cbranch_vccnz .Latt_spin25\n" \
    ".Latt_go25:\n" \
    "v_smfmac_f32_32x32x32_bf16 v[192:207], v[252:255], v[136:143], v84\n" \
    "s_add_u32 m0, s44, 57344\n" \
    "s_nop 0\n" \
    "global_load_lds_dwordx4 v92, s[58:59] sc1\n" \
    "s_add_u32 s58, s58, 1024\n" \
    "s_addc_u32 s59, s59, 0\n" \
    "v_smfmac_f32_32x32x32_bf16 v[208:223], v[248:251], v[144:151], v84\n" \
    "s_add_u32 m0, s44, 58368\n" \
    "s_nop 0\n" \
    "global_load_lds_dwordx4 v92, s[58:59] sc1\n" \
    "s_add_u32 s58, s58, 1024\n" \
    "s_addc_u32 s59, s59, 0\n" \
    "v_mfma_f32_32x32x16_bf16 v[96:111], v[236:239], v[224:227], v[96:111]\n" \
    "s_add_u32 m0, s44, 59392\n" \
    "s_nop 0\n" \
    "global_load_lds_dwordx4 v92, s[58:59] sc1\n" \
    "s_add_u32 s58, s58, 1024\n" \
    "s_addc_u32 s59, s59, 0\n" \
    "v_mfma_f32_32x32x16_bf16 v[112:127], v[236:239], v[228:231], v[112:127]\n" \
    "ds_read_b64 v[236:237], v85 offset:5376\n" \
    "ds_read_b64 v[238:239], v85 offset:5888\n" \
    "s_add_u32 m0, s44, 60416\n" \
    "s_nop 0\n" \
    "global_load_lds_dwordx4 v92, s[58:59] sc1\n" \
    "s_add_u32 s58, s58, 1024\n" \
    "s_addc_u32 s59, s59, 0\n" \
    "s_add_u32 m0, s44, 61440\n" \
    "s_nop 0\n" \
    "global_load_lds_dwordx4 v92, s[58:59] sc1\n" \
    "s_add_u32 s58, s58, 1024\n" \
    "s_addc_u32 s59, s59, 0\n" \
    "v_smfmac_f32_32x32x32_bf16 v[208:223], v[252:255], v[152:159], v84\n" \
    "s_setprio 0\n" \
    "v_exp_f32 v96, v96\n" \
    "s_setprio 1\n" \
    "v_exp_f32 v97, v97\n" \
    "v_exp_f32 v98, v98\n" \
    "v_exp_f32 v99, v99\n" \
    "v_exp_f32 v100, v100\n" \
    "v_exp_f32 v101, v101\n" \
    "v_exp_f32 v102, v102\n" \
    "v_exp_f32 v103, v103\n" \
    "v_exp_f32 v104, v104\n" \
    "v_exp_f32 v105, v105\n" \
    "v_exp_f32 v106, v106\n" \
    "v_exp_f32 v107, v107\n" \
    "v_exp_f32 v108, v108\n" \
    "v_exp_f32 v109, v109\n" \
    "v_exp_f32 v110, v110\n" \
    "v_exp_f32 v111, v111\n" \
    "v_exp_f32 v112, v112\n" \
    "v_exp_f32 v113, v113\n" \
    "v_exp_f32 v114, v114\n" \
    "v_exp_f32 v115, v115\n" \
    "v_exp_f32 v116, v116\n" \
    "v_exp_f32 v117, v117\n" \
    "v_exp_f32 v118, v118\n" \
    "v_exp_f32 v119, v119\n" \
    "v_exp_f32 v120, v120\n" \
    "v_exp_f32 v121, v121\n" \
    "v_exp_f32 v122, v122\n" \
    "v_exp_f32 v123, v123\n" \
    "v_exp_f32 v124, v124\n" \
    "v_exp_f32 v125, v125\n" \
    "v_exp_f32 v126, v126\n" \
    "v_exp_f32 v127, v127\n" \
    "s_setprio 2\n" \
    "s_waitcnt lgkmcnt(0)\n" \
    "v_mfma_f32_32x32x4_2b_bf16 v[128:159], v[234:235], v[232:233], v[160:191] cbsz:1 abid:1\n" \
    "s_mov_b64 exec, s[48:49]\n" \
    "ds_read_b128 v[248:251], v86 offset:6400\n" \
    "ds_read_b128 v[252:255], v86 offset:7040\n" \
    "s_mov_b64 exec, -1\n" \
    "s_nop 4\n" \
    "v_smfmac_f32_32x32x32_bf16 v[192:207], v[240:243], v[96:103], v84\n" \
    "global_load_dwordx2 v[34:35], v5, s[40:41] sc1\n" \
    "s_lshl_b32 s62, s62, 7\n" \
    "s_add_u32 s62, s62, 22740992\n" \
    "s_add_u32 s62, s40, s62\n" \
    "s_addc_u32 s63, s41, 0\n" \
    "v_smfmac_f32_32x32x32_bf16 v[192:207], v[244:247], v[104:111], v84\n" \
    "s_lshr_b32 s67, %[wid], 2\n" \
    "s_mul_i32 s66, s67, 25600\n" \
    "s_add_u32 s66, s66, 108544\n" \
    "v_add_u32 v7, s66, v92\n" \
    "v_smfmac_f32_32x32x32_bf16 v[208:223], v[240:243], v[112:119], v84\n" \
    "s_mul_i32 s47, s51, 5120\n" \
    "s_add_u32 s47, s47, s66\n" \
    "v_mfma_f32_32x32x16_bf16 v[128:143], v[236:239], v[224:227], v[128:143]\n" \
    "s_lshl_b32 s66, s51, 11\n" \
    "s_sub_u32 s66, s44, s66\n" \
    "v_add_u32 v8, s66, v92\n" \
    "s_mul_i32 s67, s67, 40960\n" \
    "v_mfma_f32_32x32x16_bf16 v[144:159], v[236:239], v[228:231], v[144:159]\n" \
    "ds_read_b64 v[236:237], v85 offset:10240\n" \
    "ds_read_b64 v[238:239], v85 offset:10752\n" \
    "ds_read_b64 v[234:235], v85 offset:11264\n" \
    "s_sub_u32 s67, 20480, s67\n" \
    "v_add_u32 v9, s67, v8\n" \
    "v_smfmac_f32_32x32x32_bf16 v[208:223], v[244:247], v[120:127], v84\n" \
    "s_setprio 0\n" \
    "v_exp_f32 v128, v128\n" \
    "s_setprio 1\n" \
    "v_exp_f32 v129, v129\n" \
    "v_exp_f32 v130, v130\n" \
    "v_exp_f32 v131, v131\n" \
    "v_exp_f32 v132, v132\n" \
    "v_exp_f32 v133, v133\n" \
    "v_exp_f32 v134, v134\n" \
    "v_exp_f32 v135, v135\n" \
    "v_exp_f32 v136, v136\n" \
    "v_exp_f32 v137, v137\n" \
    "v_exp_f32 v138, v138\n" \
    "v_exp_f32 v139, v139\n" \
    "v_exp_f32 v140, v140\n" \
    "v_exp_f32 v141, v141\n" \
    "v_exp_f32 v142, v142\n" \
    "v_exp_f32 v143, v143\n" \
    "v_exp_f32 v144, v144\n" \
    "v_exp_f32 v145, v145\n" \
    "v_exp_f32 v146, v146\n" \
    "v_exp_f32 v147, v147\n" \
    "v_exp_f32 v148, v148\n" \
    "v_exp_f32 v149, v149\n" \
    "v_exp_f32 v150, v150\n" \
    "v_exp_f32 v151, v151\n" \
    "v_exp_f32 v152, v152\n" \
    "v_exp_f32 v153, v153\n" \
    "v_exp_f32 v154, v154\n" \
    "v_exp_f32 v155, v155\n" \
    "v_exp_f32 v156, v156\n" \
    "v_exp_f32 v157, v157\n" \
    "v_exp_f32 v158, v158\n" \
    "v_exp_f32 v159, v159\n" \
    "s_setprio 2\n" \
    "s_waitcnt lgkmcnt(0)\n" \
    "v_mfma_f32_32x32x4_2b_bf16 v[96:127], v[234:235], v[232:233], v[160:191] cbsz:1 abid:0\n" \
    "s_waitcnt vmcnt(0)\n" \
    "v_cmp_ne_u64 vcc, %[tag], v[34:35]\n" \
    "s_mov_b32 s50, 0x10000\n" \
    "s_cbranch_vccnz .Latt_fspin27\n" \
    ".Latt_fgo27:\n" \
    "s_mov_b64 exec, s[48:49]\n" \
    "ds_read_b128 v[240:243], v86 offset:10240\n" \
    "ds_read_b128 v[244:247], v86 offset:10880\n" \
    "s_mov_b64 exec, -1\n" \
    "s_nop 0\n" \
    "v_smfmac_f32_32x32x32_bf16 v[192:207], v[248:251], v[128:135], v84\n" \
    "global_load_dwordx4 v[44:47], v6, s[60:61] sc1\n" \
    "s_add_u32 s60, s60, 4096\n" \
    "s_addc_u32 s61, s61, 0\n" \
    "v_smfmac_f32_32x32x32_bf16 v[192:207], v[252:255], v[136:143], v84\n" \
    "global_load_dwordx4 v[48:51], v6, s[60:61] sc1\n" \
    "s_add_u32 s60, s60, 4096\n" \
    "s_addc_u32 s61, s61, 0\n" \
    "v_smfmac_f32_32x32x32_bf16 v[208:223], v[248:251], v[144:151], v84\n" \
    "global_load_dwordx4 v[52:55], v6, s[60:61] sc1\n" \
    "s_add_u32 s60, s60, 4096\n" \
    "s_addc_u32 s61, s61, 0\n" \
    "v_mfma_f32_32x32x16_bf16 v[96:111], v[236:239], v[224:227], v[96:111]\n" \
    "global_load_dwordx4 v[56:59], v6, s[60:61] sc1\n" \
    "s_add_u32 s60, s60, 4096\n" \
    "s_addc_u32 s61, s61, 0\n" \
    "s_nop 0\n" \
    "v_mfma_f32_32x32x16_bf16 v[112:127], v[236:239], v[228:231], v[112:127]\n" \
    "ds_read_b64 v[236:237], v85 offset:10496\n" \
    "ds_read_b64 v[238:239], v85 offset:11008\n" \
    "global_load_dwordx4 v[60:63], v6, s[60:61] sc1\n" \
    "s_add_u32 s60, s60, 4096\n" \
    "s_addc_u32 s61, s61, 0\n" \
    "v_smfmac_f32_32x32x32_bf16 v[208:223], v[252:255], v[152:159], v84\n" \
    "s_setprio 0\n" \
    "v_exp_f32 v96, v96\n" \
    "s_setprio 1\n" \
    "v_exp_f32 v97, v97\n" \
    "v_exp_f32 v98, v98\n" \
    "v_exp_f32 v99, v99\n" \
    "v_exp_f32 v100, v100\n" \
    "v_exp_f32 v101, v101\n" \
    "v_exp_f32 v102, v102\n" \
    "v_exp_f32 v103, v103\n" \
    "v_exp_f32 v104, v104\n" \
    "v_exp_f32 v105, v105\n" \
    "v_exp_f32 v106, v106\n" \
    "v_exp_f32 v107, v107\n" \
    "v_exp_f32 v108, v108\n" \
    "v_exp_f32 v109, v109\n" \
    "v_exp_f32 v110, v110\n" \
    "v_exp_f32 v111, v111\n" \
    "v_exp_f32 v112, v112\n" \
    "v_exp_f32 v113, v113\n" \
    "v_exp_f32 v114, v114\n" \
    "v_exp_f32 v115, v115\n" \
    "v_exp_f32 v116, v116\n" \
    "v_exp_f32 v117, v117\n" \
    "v_exp_f32 v118, v118\n" \
    "v_exp_f32 v119, v119\n" \
    "v_exp_f32 v120, v120\n" \
    "v_exp_f32 v121, v121\n" \
    "v_exp_f32 v122, v122\n" \
    "v_exp_f32 v123, v123\n" \
    "v_exp_f32 v124, v124\n" \
    "v_exp_f32 v125, v125\n" \
    "v_exp_f32 v126, v126\n" \
    "v_exp_f32 v127, v127\n" \
    "s_setprio 2\n" \
    "s_waitcnt lgkmcnt(0)\n" \
    "v_mfma_f32_32x32x4_2b_bf16 v[128:159], v[234:235], v[232:233], v[160:191] cbsz:1 abid:1\n" \
    "s_add_u32 s46, s46, 1\n" \
    "v_mov_b32 v95, s46\n" \
    "ds_write_b32 v93, v95\n" \
    "s_mov_b64 exec, s[48:49]\n" \
    "ds_read_b128 v[248:251], v86 offset:11520\n" \
    "ds_read_b128 v[252:255], v86 offset:12160\n" \
    "s_mov_b64 exec, -1\n" \
    "s_nop 1\n" \
    "v_smfmac_f32_32x32x32_bf16 v[192:207], v[240:243], v[96:103], v84\n" \
    "global_load_dwordx4 v[64:67], v6, s[60:61] sc1\n" \
    "s_add_u32 s60, s60, 4096\n" \
    "s_addc_u32 s61, s61, 0\n" \
    "v_smfmac_f32_32x32x32_bf16 v[192:207], v[244:247], v[104:111], v84\n" \
    "global_load_dwordx4 v[68:71], v6, s[60:61] sc1\n" \
    "s_add_u32 s60, s60, 4096\n" \
    "s_addc_u32 s61, s61, 0\n" \
    "v_smfmac_f32_32x32x32_bf16 v[208:223], v[240:243], v[112:119], v84\n" \
    "global_load_dwordx4 v[72:75], v6, s[60:61] sc1\n" \
    "s_add_u32 s60, s60, 4096\n" \
    "s_addc_u32 s61, s61, 0\n" \
    "v_mfma_f32_32x32x16_bf16 v[128:143], v[236:239], v[224:227], v[128:143]\n" \
    "global_load_dwordx4 v[76:79], v6, s[60:61] sc1\n" \
    "s_add_u32 s60, s60, 4096\n" \
    "s_addc_u32 s61, s61, 0\n" \
    "s_nop 0\n" \
    "v_mfma_f32_32x32x16_bf16 v[144:159], v[236:239], v[228:231], v[144:159]\n" \
    "ds_read_b64 v[236:237], v85 offset:15360\n" \
    "ds_read_b64 v[238:239], v85 offset:15872\n" \
    "ds_read_b64 v[234:235], v85 offset:16384\n" \
    "global_load_dwordx4 v[80:83], v6, s[60:61] sc1\n" \
    "s_add_u32 s60, s60, 4096\n" \
    "s_addc_u32 s61, s61, 0\n" \
    "v_smfmac_f32_32x32x32_bf16 v[208:223], v[244:247], v[120:127], v84\n" \
    "s_setprio 0\n" \
    "v_exp_f32 v128, v128\n" \
    "s_setprio 1\n" \
    "v_exp_f32 v129, v129\n" \
    "v_exp_f32 v130, v130\n" \
    "v_exp_f32 v131, v131\n" \
    "v_exp_f32 v132, v132\n" \
    "v_exp_f32 v133, v133\n" \
    "v_exp_f32 v134, v134\n" \
    "v_exp_f32 v135, v135\n" \
    "v_exp_f32 v136, v136\n" \
    "v_exp_f32 v137, v137\n" \
    "v_exp_f32 v138, v138\n" \
    "v_exp_f32 v139, v139\n" \
    "v_exp_f32 v140, v140\n" \
    "v_exp_f32 v141, v141\n" \
    "v_exp_f32 v142, v142\n" \
    "v_exp_f32 v143, v143\n" \
    "v_exp_f32 v144, v144\n" \
    "v_exp_f32 v145, v145\n" \
    "v_exp_f32 v146, v146\n" \
    "v_exp_f32 v147, v147\n" \
    "v_exp_f32 v148, v148\n" \
    "v_exp_f32 v149, v149\n" \
    "v_exp_f32 v150, v150\n" \
    "v_exp_f32 v151, v151\n" \
    "v_exp_f32 v152, v152\n" \
    "v_exp_f32 v153, v153\n" \
    "v_exp_f32 v154, v154\n" \
    "v_exp_f32 v155, v155\n" \
    "v_exp_f32 v156, v156\n" \
    "v_exp_f32 v157, v157\n" \
    "v_exp_f32 v158, v158\n" \
    "v_exp_f32 v159, v159\n" \
    "s_setprio 2\n" \
    "s_waitcnt lgkmcnt(0)\n" \
    "v_mfma_f32_32x32x4_2b_bf16 v[96:127], v[234:235], v[232:233], v[160:191] cbsz:1 abid:0\n" \
    "s_mov_b64 exec, s[48:49]\n" \
    "ds_read_b128 v[240:243], v86 offset:15360\n" \
    "ds_read_b128 v[244:247], v86 offset:16000\n" \
    "s_mov_b64 exec, -1\n" \
    "s_nop 4\n" \
    "v_smfmac_f32_32x32x32_bf16 v[192:207], v[248:251], v[128:135], v84\n" \
    "s_lshr_b32 s66, %[head], 1\n" \
    "s_mul_i32 s66, s66, 10\n" \
    "s_lshr_b32 s67, %[wid], 2\n" \
    "s_add_u32 s66, s66, s67\n" \
    "s_lshl_b32 s66, s66, 9\n" \
    "v_smfmac_f32_32x32x32_bf16 v[192:207], v[252:255], v[136:143], v84\n" \
    "s_add_u32 s66, s66, s45\n" \
    "s_add_u32 s66, s66, 18350080\n" \
    "s_and_b32 s67, %[head], 1\n" \
    "s_lshl_b32 s64, s67, 11\n" \
    "s_add_u32 s64, s64, s66\n" \
    "v_smfmac_f32_32x32x32_bf16 v[208:223], v[248:251], v[144:151], v84\n" \
    "s_add_u32 s54, s40, s64\n" \
    "s_addc_u32 s55, s41, 0\n" \
    "s_lshl_b32 s67, s67, 3\n" \
    "s_add_u32 s66, s66, s67\n" \
    "s_add_u32 s66, s66, 4096\n" \
    "v_mfma_f32_32x32x16_bf16 v[96:111], v[236:239], v[224:227], v[96:111]\n" \
    "s_add_u32 s56, s40, s66\n" \
    "s_addc_u32 s57, s41, 0\n" \
    "v_and_b32 v10, 0x200, v92\n" \
    "v_add_u32 v10, v10, v92\n" \
    "v_mfma_f32_32x32x16_bf16 v[112:127], v[236:239], v[228:231], v[112:127]\n" \
    "ds_read_b64 v[236:237], v85 offset:15616\n" \
    "ds_read_b64 v[238:239], v85 offset:16128\n" \
    "s_add_u32 s66, %[wa], 824320\n" \
    "s_add_u32 s58, s40, s66\n" \
    "s_addc_u32 s59, s41, 0\n" \
    "v_smfmac_f32_32x32x32_bf16 v[208:223], v[252:255], v[152:159], v84\n" \
    "s_setprio 0\n" \
    "v_exp_f32 v96, v96\n" \
    "s_setprio 1\n" \
    "v_exp_f32 v97, v97\n" \
    "v_exp_f32 v98, v98\n" \
    "v_exp_f32 v99, v99\n" \
    "v_exp_f32 v100, v100\n" \
    "v_exp_f32 v101, v101\n" \
    "v_exp_f32 v102, v102\n" \
    "v_exp_f32 v103, v103\n" \
    "v_exp_f32 v104, v104\n" \
    "v_exp_f32 v105, v105\n" \
    "v_exp_f32 v106, v106\n" \
    "v_exp_f32 v107, v107\n" \
    "v_exp_f32 v108, v108\n" \
    "v_exp_f32 v109, v109\n" \
    "v_exp_f32 v110, v110\n" \
    "v_exp_f32 v111, v111\n" \
    "v_exp_f32 v112, v112\n" \
    "v_exp_f32 v113, v113\n" \
    "v_exp_f32 v114, v114\n" \
    "v_exp_f32 v115, v115\n" \
    "v_exp_f32 v116, v116\n" \
    "v_exp_f32 v117, v117\n" \
    "v_exp_f32 v118, v118\n" \
    "v_exp_f32 v119, v119\n" \
    "v_exp_f32 v120, v120\n" \
    "v_exp_f32 v121, v121\n" \
    "v_exp_f32 v122, v122\n" \
    "v_exp_f32 v123, v123\n" \
    "v_exp_f32 v124, v124\n" \
    "v_exp_f32 v125, v125\n" \
    "v_exp_f32 v126, v126\n" \
    "v_exp_f32 v127, v127\n" \
    "s_setprio 2\n" \
    "s_waitcnt lgkmcnt(0)\n" \
    "v_mfma_f32_32x32x4_2b_bf16 v[128:159], v[234:235], v[232:233], v[160:191] cbsz:1 abid:1\n" \
    "ds_read_b32 v95, v94\n" \
    "s_mov_b64 exec, s[48:49]\n" \
    "ds_read_b128 v[248:251], v86 offset:16640\n" \
    "ds_read_b128 v[252:255], v86 offset:17280\n" \
    "s_mov_b64 exec, -1\n" \
    "s_nop 3\n" \
    "v_smfmac_f32_32x32x32_bf16 v[192:207], v[240:243], v[96:103], v84\n" \
    "s_waitcnt lgkmcnt(2)\n" \
    "v_cmp_gt_u32 vcc, s46, v95\n" \
    "s_movk_i32 s50, 0x4000\n" \
    "s_cbranch_vccnz .Latt_spin30\n" \
    ".Latt_go30:\n" \
    "s_add_u32 s60, s60, 2580480\n" \
    "s_addc_u32 s61, s61, 0\n" \
    "v_smfmac_f32_32x32x32_bf16 v[192:207], v[244:247], v[104:111], v84\n" \
    "ds_read_b128 v[160:163], v7 offset:0\n" \
    "ds_read_b128 v[164:167], v7 offset:1024\n" \
    "ds_read_b128 v[168:171], v7 offset:2048\n" \
    "v_smfmac_f32_32x32x32_bf16 v[208:223], v[240:243], v[112:119], v84\n" \
    "ds_read_b128 v[172:175], v7 offset:3072\n" \
    "ds_read_b128 v[176:179], v7 offset:4096\n" \
    "ds_read_b128 v[180:183], v7 offset:5120\n" \
    "v_mfma_f32_32x32x16_bf16 v[128:143], v[236:239], v[224:227], v[128:143]\n" \
    "ds_read_b128 v[184:187], v7 offset:6144\n" \
    "ds_read_b128 v[188:191], v7 offset:7168\n" \
    "ds_read_b128 v[96:99], v7 offset:8192\n" \
    "ds_read_b128 v[100:103], v7 offset:9216\n" \
    "v_mfma_f32_32x32x16_bf16 v[144:159], v[236:239], v[228:231], v[144:159]\n" \
    "ds_read_b128 v[104:107], v7 offset:10240\n" \
    "ds_read_b128 v[108:111], v7 offset:11264\n" \
    "ds_read_b128 v[112:115], v7 offset:12288\n" \
    "ds_read_b128 v[116:119], v7 offset:13312\n" \
    "s_nop 0\n" \
    "v_smfmac_f32_32x32x32_bf16 v[208:223], v[244:247], v[120:127], v84\n" \
    "ds_read_b128 v[120:123], v7 offset:14336\n" \
    "ds_read_b128 v[124:127], v7 offset:15360\n" \
    "ds_read_b128 v[244:247], v7 offset:16384\n" \
    "s_setprio 0\n" \
    "v_exp_f32 v128, v128\n" \
    "s_setprio 1\n" \
    "v_exp_f32 v129, v129\n" \
    "v_exp_f32 v130, v130\n" \
    "v_exp_f32 v131, v131\n" \
    "v_exp_f32 v132, v132\n" \
    "v_exp_f32 v133, v133\n" \
    "v_exp_f32 v134, v134\n" \
    "v_exp_f32 v135, v135\n" \
    "v_exp_f32 v136, v136\n" \
    "v_exp_f32 v137, v137\n" \
    "v_exp_f32 v138, v138\n" \
    "v_exp_f32 v139, v139\n" \
    "v_exp_f32 v140, v140\n" \
    "v_exp_f32 v141, v141\n" \
    "v_exp_f32 v142, v142\n" \
    "v_exp_f32 v143, v143\n" \
    "v_exp_f32 v144, v144\n" \
    "v_exp_f32 v145, v145\n" \
    "v_exp_f32 v146, v146\n" \
    "v_exp_f32 v147, v147\n" \
    "v_exp_f32 v148, v148\n" \
    "v_exp_f32 v149, v149\n" \
    "v_exp_f32 v150, v150\n" \
    "v_exp_f32 v151, v151\n" \
    "v_exp_f32 v152, v152\n" \
    "v_exp_f32 v153, v153\n" \
    "v_exp_f32 v154, v154\n" \
    "v_exp_f32 v155, v155\n" \
    "v_exp_f32 v156, v156\n" \
    "v_exp_f32 v157, v157\n" \
    "v_exp_f32 v158, v158\n" \
    "v_exp_f32 v159, v159\n" \
    "s_setprio 2\n" \
    "s_waitcnt lgkmcnt(0)\n" \
    "v_smfmac_f32_32x32x32_bf16 v[208:223], v[248:251], v[144:151], v84\n" \
    "v_smfmac_f32_32x32x32_bf16 v[208:223], v[252:255], v[152:159], v84\n" \
    "v_smfmac_f32_32x32x32_bf16 v[192:207], v[248:251], v[128:135], v84\n" \
    "v_smfmac_f32_32x32x32_bf16 v[192:207], v[252:255], v[136:143], v84\n" \
    "ds_read_b128 v[128:131], v7 offset:17408\n" \
    "ds_read_b128 v[132:135], v7 offset:18432\n" \
    "ds_read_b128 v[136:139], v7 offset:19456\n" \
    "ds_read_b128 v[140:143], v7 offset:20480\n" \
    "ds_read_b128 v[144:147], v7 offset:21504\n" \
    "ds_read_b128 v[148:151], v7 offset:22528\n" \
    "ds_read_b128 v[152:155], v7 offset:23552\n" \
    "ds_read_b128 v[156:159], v7 offset:24576\n" \
    "s_nop 1\n" \
    "ds_write_b128 v8, v[208:211] offset:0\n" \
    "ds_write_b128 v8, v[212:215] offset:1024\n" \
    "ds_write_b128 v8, v[216:219] offset:2048\n" \
    ".Lis4: s_cbranch_execz .Lis_ret\n" \
    "s_waitcnt vmcnt(0) lgkmcnt(0)\n" \
    "s_barrier\n" \
    "ds_read_b128 v[28:31], v9 offset:2048\n" \
    "ds_read_b128 v[20:23], v9 offset:0\n" \
    "ds_read_b128 v[24:27], v9 offset:1024\n" \
    "s_waitcnt lgkmcnt(2)\n" \
    "v_add_f32 v200, v200, v28\n" \
    "v_add_f32 v201, v201, v29\n" \
    "v_add_f32 v202, v202, v30\n" \
    "v_add_f32 v203, v203, v31\n" \
    "s_waitcnt lgkmcnt(1)\n" \
    "v_add_f32 v192, v192, v20\n" \
    "v_add_f32 v193, v193, v21\n" \
    "v_add_f32 v194, v194, v22\n" \
    "v_add_f32 v195, v195, v23\n" \
    "s_waitcnt lgkmcnt(0)\n" \
    "v_add_f32 v196, v196, v24\n" \
    "v_add_f32 v197, v197, v25\n" \
    "v_add_f32 v198, v198, v26\n" \
    "v_add_f32 v199, v199, v27\n" \
    "v_add_f32_e64 v20, |v192|, |v193|\n" \
    "v_add_f32_e64 v21, |v194|, |v195|\n" \
    "v_add_f32_e64 v22, |v196|, |v197|\n" \
    "v_add_f32_e64 v23, |v198|, |v199|\n" \
    "v_add_f32_e64 v24, |v200|, |v201|\n" \
    "v_add_f32_e64 v25, |v202|, |v203|\n" \
    "v_mov_b32 v204, v200\n" \
    "v_mov_b32 v205, v200\n" \
    "v_add_f32 v20, v20, v21\n" \
    "v_add_f32 v22, v22, v23\n" \
    "v_add_f32 v24, v24, v25\n" \
    "v_permlane32_swap_b32 v205, v204\n" \
    "v_add_f32 v20, v20, v22\n" \
    "v_add_f32 v20, v20, v24\n" \
    "v_cmp_ngt_f32 vcc, 0x7149f2ca, v20\n" \
    "s_nop 0\n" \
    "s_or_b32 s50, vcc_lo, vcc_hi\n"
#define OUT_ALL \
    "v_rcp_f32 v204, v204\n" \
    "s_lshr_b32 s67, %[wid], 2\n" \
    "v_mul_f32 v192, v204, v192\n" \
    "v_mul_f32 v193, v204, v193\n" \
    "v_mul_f32 v194, v204, v194\n" \
    "v_mul_f32 v195, v204, v195\n" \
    "v_mul_f32 v196, v204, v196\n" \
    "v_mul_f32 v197, v204, v197\n" \
    "v_mul_f32 v198, v204, v198\n" \
    "v_mul_f32 v199, v204, v199\n" \
    "v_mul_f32 v200, v204, v200\n" \
    "v_mul_f32 v201, v204, v201\n" \
    "v_mul_f32 v202, v204, v202\n" \
    "v_mul_f32 v203, v204, v203\n" \
    "v_cvt_pk_bf16_f32 v192, v192, v193\n" \
    "v_cvt_pk_bf16_f32 v193, v194, v195\n" \
    "v_cvt_pk_bf16_f32 v194, v196, v197\n" \
    "v_cvt_pk_bf16_f32 v195, v198, v199\n" \
    "v_cvt_pk_bf16_f32 v196, v200, v201\n" \
    "v_cvt_pk_bf16_f32 v197, v202, v203\n" \
    "global_store_dwordx4 v10, v[192:195], s[54:55] sc1\n" \
    "s_mov_b32 exec_hi, 0\n" \
    "global_store_dwordx2 v10, v[196:197], s[56:57] sc1\n" \
    "s_mov_b32 exec_hi, -1\n" \
    "s_mul_i32 s66, s67, 51200\n" \
    "v_add_u32 v11, s66, v92\n" \
    "s_mul_i32 s66, s67, 20480\n" \
    "s_add_u32 s66, s66, 67584\n" \
    "v_add_u32 v12, s66, v92\n" \
    "s_add_u32 m0, s47, 0\n" \
    "s_nop 0\n" \
    "global_load_lds_dwordx4 v92, s[58:59] offset:0 sc1\n" \
    "global_load_lds_dwordx4 v92, s[58:59] offset:1024 sc1\n" \
    "global_load_lds_dwordx4 v92, s[58:59] offset:2048 sc1\n" \
    "global_load_lds_dwordx4 v92, s[58:59] offset:3072 sc1\n" \
    "v_mfma_f32_16x16x32_bf16 v[224:227], v[160:163], v[44:47], 0\n" \
    "v_mfma_f32_16x16x32_bf16 v[228:231], v[164:167], v[44:47], 0\n" \
    "ds_read_b128 v[160:163], v11 offset:0\n" \
    "v_mfma_f32_16x16x32_bf16 v[232:235], v[168:171], v[44:47], 0\n" \
    "ds_read_b128 v[164:167], v11 offset:1024\n" \
    "v_mfma_f32_16x16x32_bf16 v[236:239], v[172:175], v[44:47], 0\n" \
    "ds_read_b128 v[168:171], v11 offset:2048\n" \
    "v_mfma_f32_16x16x32_bf16 v[240:243], v[176:179], v[44:47], 0\n" \
    "ds_read_b128 v[172:175], v11 offset:3072\n" \
    "ds_read_b128 v[176:179], v11 offset:4096\n" \
    "s_add_u32 m0, s47, 4096\n" \
    "s_add_u32 s58, s58, 4096\n" \
    "s_addc_u32 s59, s59, 0\n" \
    "global_load_lds_dwordx4 v92, s[58:59] offset:0 sc1\n" \
    "s_add_u32 s58, s58, 21504\n" \
    "s_addc_u32 s59, s59, 0\n" \
    "v_mov_b64 v[34:35], %[tag]\n" \
    "v_mfma_f32_16x16x32_bf16 v[224:227], v[180:183], v[48:51], v[224:227]\n" \
    "v_mfma_f32_16x16x32_bf16 v[228:231], v[184:187], v[48:51], v[228:231]\n" \
    "ds_read_b128 v[180:183], v11 offset:5120\n" \
    "v_mfma_f32_16x16x32_bf16 v[232:235], v[188:191], v[48:51], v[232:235]\n" \
    "ds_read_b128 v[184:187], v11 offset:6144\n" \
    "v_mfma_f32_16x16x32_bf16 v[236:239], v[96:99], v[48:51], v[236:239]\n" \
    "ds_read_b128 v[188:191], v11 offset:7168\n" \
    "v_mfma_f32_16x16x32_bf16 v[240:243], v[100:103], v[48:51], v[240:243]\n" \
    "ds_read_b128 v[96:99], v11 offset:8192\n" \
    "ds_read_b128 v[100:103], v11 offset:9216\n" \
    "s_and_b32 s66, %[b], 7\n" \
    "s_lshl_b32 s66, s66, 2\n" \
    "s_lshr_b32 s67, %[b], 6\n" \
    "s_add_u32 s66, s66, s67\n" \
    "s_lshl_b32 s67, s66, 13\n" \
    "s_add_u32 s67, s67, 22740992\n" \
    "s_add_u32 s54, s40, s67\n" \
    "s_addc_u32 s55, s41, 0\n" \
    "v_lshlrev_b32 v14, 3, v92\n" \
    "s_cmp_eq_u32 %[wid], 7\n" \
    "s_cselect_b64 s[52:53], -1, 0\n" \
    "v_mfma_f32_16x16x32_bf16 v[224:227], v[104:107], v[52:55], v[224:227]\n" \
    "v_mfma_f32_16x16x32_bf16 v[228:231], v[108:111], v[52:55], v[228:231]\n" \
    "ds_read_b128 v[104:107], v12 offset:0\n" \
    "v_mfma_f32_16x16x32_bf16 v[232:235], v[112:115], v[52:55], v[232:235]\n" \
    "ds_read_b128 v[108:111], v12 offset:1024\n" \
    "v_mfma_f32_16x16x32_bf16 v[236:239], v[116:119], v[52:55], v[236:239]\n" \
    "ds_read_b128 v[112:115], v12 offset:2048\n" \
    "v_mfma_f32_16x16x32_bf16 v[240:243], v[120:123], v[52:55], v[240:243]\n" \
    "ds_read_b128 v[116:119], v12 offset:3072\n" \
    "ds_read_b128 v[120:123], v12 offset:4096\n" \
    "s_and_b32 s51, %[wid], 3\n" \
    "s_lshl_b32 s66, s66, 2\n" \
    "s_add_u32 s66, s66, s51\n" \
    "s_lshl_b32 s67, s66, 6\n" \
    "s_add_u32 s67, s67, 22609920\n" \
    "s_add_u32 s56, s40, s67\n" \
    "s_addc_u32 s57, s41, 0\n" \
    "s_mul_i32 s66, s66, 20480\n" \
    "s_bfe_u32 s67, %[b], 0x30003\n" \
    "s_mul_i32 s67, s67, 160\n" \
    "s_add_u32 s66, s66, s67\n" \
    "s_mov_b64 s[64:65], %[out]\n" \
    "s_add_u32 s64, s64, s66\n" \
    "s_addc_u32 s65, s65, 0\n" \
    "s_waitcnt lgkmcnt(10)\n" \
    "v_mfma_f32_16x16x32_bf16 v[224:227], v[124:127], v[56:59], v[224:227]\n" \
    "v_mfma_f32_16x16x32_bf16 v[228:231], v[244:247], v[56:59], v[228:231]\n" \
    "ds_read_b128 v[124:127], v12 offset:5120\n" \
    "v_mfma_f32_16x16x32_bf16 v[232:235], v[128:131], v[56:59], v[232:235]\n" \
    "ds_read_b128 v[244:247], v12 offset:6144\n" \
    "v_mfma_f32_16x16x32_bf16 v[236:239], v[132:135], v[56:59], v[236:239]\n" \
    "ds_read_b128 v[128:131], v12 offset:7168\n" \
    "v_mfma_f32_16x16x32_bf16 v[240:243], v[136:139], v[56:59], v[240:243]\n" \
    "ds_read_b128 v[132:135], v12 offset:8192\n" \
    "ds_read_b128 v[136:139], v12 offset:9216\n" \
    "s_waitcnt vmcnt(5)\n" \
    "s_mov_b64 exec, 1\n" \
    "global_store_dwordx2 v92, v[34:35], s[62:63] sc1\n" \
    "s_mov_b64 exec, -1\n" \
    "s_waitcnt lgkmcnt(10)\n" \
    "v_mfma_f32_16x16x32_bf16 v[224:227], v[140:143], v[60:63], v[224:227]\n" \
    "v_mfma_f32_16x16x32_bf16 v[228:231], v[144:147], v[60:63], v[228:231]\n" \
    "ds_read_b128 v[140:143], v12 offset:10240\n" \
    "v_mfma_f32_16x16x32_bf16 v[232:235], v[148:151], v[60:63], v[232:235]\n" \
    "ds_read_b128 v[144:147], v12 offset:11264\n" \
    "v_mfma_f32_16x16x32_bf16 v[236:239], v[152:155], v[60:63], v[236:239]\n" \
    "ds_read_b128 v[148:151], v12 offset:12288\n" \
    "v_mfma_f32_16x16x32_bf16 v[240:243], v[156:159], v[60:63], v[240:243]\n" \
    "ds_read_b128 v[152:155], v12 offset:13312\n" \
    "ds_read_b128 v[156:159], v12 offset:14336\n" \
    "s_waitcnt vmcnt(1)\n" \
    "s_barrier\n" \
    "s_add_u32 m0, s44, 0\n" \
    "s_nop 0\n" \
    "global_load_lds_dwordx4 v92, s[58:59] offset:0 sc1\n" \
    "global_load_lds_dwordx4 v92, s[58:59] offset:1024 sc1\n" \
    "global_load_lds_dwordx4 v92, s[58:59] offset:2048 sc1\n" \
    "global_load_lds_dwordx4 v92, s[58:59] offset:3072 sc1\n" \
    "s_waitcnt lgkmcnt(10)\n" \
    "v_mfma_f32_16x16x32_bf16 v[224:227], v[160:163], v[64:67], v[224:227]\n" \
    "v_mfma_f32_16x16x32_bf16 v[228:231], v[164:167], v[64:67], v[228:231]\n" \
    "ds_read_b128 v[160:163], v12 offset:15360\n" \
    "v_mfma_f32_16x16x32_bf16 v[232:235], v[168:171], v[64:67], v[232:235]\n" \
    "ds_read_b128 v[164:167], v12 offset:16384\n" \
    "v_mfma_f32_16x16x32_bf16 v[236:239], v[172:175], v[64:67], v[236:239]\n" \
    "ds_read_b128 v[168:171], v12 offset:17408\n" \
    "v_mfma_f32_16x16x32_bf16 v[240:243], v[176:179], v[64:67], v[240:243]\n" \
    "ds_read_b128 v[172:175], v12 offset:18432\n" \
    "ds_read_b128 v[176:179], v12 offset:19456\n" \
    "s_add_u32 m0, s44, 4096\n" \
    "s_add_u32 s58, s58, 4096\n" \
    "s_addc_u32 s59, s59, 0\n" \
    "global_load_lds_dwordx4 v92, s[58:59] offset:0 sc1\n" \
    "s_lshl_b32 s66, s51, 12\n" \
    "s_sub_u32 s47, s47, s66\n" \
    "s_add_u32 s66, s66, 9216\n" \
    "s_sub_u32 s58, s58, s66\n" \
    "s_subb_u32 s59, s59, 0\n" \
    "s_add_u32 m0, s47, 20480\n" \
    "s_lshl_b32 s66, s51, 10\n" \
    "global_load_lds_dwordx4 v92, s[58:59] sc1\n" \
    "s_sub_u32 s66, 4096, s66\n" \
    "s_add_u32 s58, s58, s66\n" \
    "s_addc_u32 s59, s59, 0\n" \
    "s_add_u32 m0, m0, s66\n" \
    "s_nop 0\n" \
    "global_load_lds_dwordx4 v92, s[58:59] sc1\n" \
    "s_waitcnt lgkmcnt(10)\n" \
    "v_mfma_f32_16x16x32_bf16 v[224:227], v[180:183], v[68:71], v[224:227]\n" \
    "v_mfma_f32_16x16x32_bf16 v[228:231], v[184:187], v[68:71], v[228:231]\n" \
    "ds_read_b128 v[180:183], v7 offset:0\n" \
    "v_mfma_f32_16x16x32_bf16 v[232:235], v[188:191], v[68:71], v[232:235]\n" \
    "ds_read_b128 v[184:187], v7 offset:1024\n" \
    "v_mfma_f32_16x16x32_bf16 v[236:239], v[96:99], v[68:71], v[236:239]\n" \
    "ds_read_b128 v[188:191], v7 offset:2048\n" \
    "v_mfma_f32_16x16x32_bf16 v[240:243], v[100:103], v[68:71], v[240:243]\n" \
    "ds_read_b128 v[96:99], v7 offset:3072\n" \
    "ds_read_b128 v[100:103], v7 offset:4096\n" \
    ".Lis5: s_cbranch_execz .Lis_ret\n" \
    "v_bfe_u32 v15, v92, 4, 4\n" \
    "v_mul_u32_u24 v16, 0x500, v15\n" \
    "v_lshlrev_b32 v15, 2, v15\n" \
    "v_subrev_u32 v13, 57344, v12\n" \
    "s_waitcnt lgkmcnt(10)\n" \
    "v_mfma_f32_16x16x32_bf16 v[224:227], v[104:107], v[72:75], v[224:227]\n" \
    "v_mfma_f32_16x16x32_bf16 v[228:231], v[108:111], v[72:75], v[228:231]\n" \
    "ds_read_b128 v[104:107], v7 offset:5120\n" \
    "v_mfma_f32_16x16x32_bf16 v[232:235], v[112:115], v[72:75], v[232:235]\n" \
    "ds_read_b128 v[108:111], v7 offset:6144\n" \
    "v_mfma_f32_16x16x32_bf16 v[236:239], v[116:119], v[72:75], v[236:239]\n" \
    "ds_read_b128 v[112:115], v7 offset:7168\n" \
    "v_mfma_f32_16x16x32_bf16 v[240:243], v[120:123], v[72:75], v[240:243]\n" \
    "ds_read_b128 v[116:119], v7 offset:8192\n" \
    "ds_read_b128 v[120:123], v7 offset:9216\n" \
    "s_waitcnt lgkmcnt(10)\n" \
    "v_mfma_f32_16x16x32_bf16 v[224:227], v[124:127], v[76:79], v[224:227]\n" \
    "v_mfma_f32_16x16x32_bf16 v[228:231], v[244:247], v[76:79], v[228:231]\n" \
    "ds_read_b128 v[124:127], v7 offset:10240\n" \
    "v_mfma_f32_16x16x32_bf16 v[232:235], v[128:131], v[76:79], v[232:235]\n" \
    "ds_read_b128 v[244:247], v7 offset:11264\n" \
    "v_mfma_f32_16x16x32_bf16 v[236:239], v[132:135], v[76:79], v[236:239]\n" \
    "ds_read_b128 v[128:131], v7 offset:12288\n" \
    "v_mfma_f32_16x16x32_bf16 v[240:243], v[136:139], v[76:79], v[240:243]\n" \
    "ds_read_b128 v[132:135], v7 offset:13312\n" \
    "ds_read_b128 v[136:139], v7 offset:14336\n" \
    "s_mov_b64 exec, s[52:53]\n" \
    "global_load_dwordx2 v[34:35], v14, s[54:55] sc1\n" \
    "s_mov_b64 exec, -1\n" \
    "s_waitcnt lgkmcnt(10)\n" \
    "v_mfma_f32_16x16x32_bf16 v[224:227], v[140:143], v[80:83], v[224:227]\n" \
    "v_mfma_f32_16x16x32_bf16 v[228:231], v[144:147], v[80:83], v[228:231]\n" \
    "ds_read_b128 v[140:143], v7 offset:15360\n" \
    "v_mfma_f32_16x16x32_bf16 v[232:235], v[148:151], v[80:83], v[232:235]\n" \
    "ds_read_b128 v[144:147], v7 offset:16384\n" \
    "v_mfma_f32_16x16x32_bf16 v[236:239], v[152:155], v[80:83], v[236:239]\n" \
    "ds_read_b128 v[148:151], v7 offset:17408\n" \
    "v_mfma_f32_16x16x32_bf16 v[240:243], v[156:159], v[80:83], v[240:243]\n" \
    "ds_read_b128 v[152:155], v7 offset:18432\n" \
    "ds_read_b128 v[156:159], v7 offset:19456\n" \
    "s_cmp_lg_u32 %[wid], 7\n" \
    "s_cbranch_scc1 2f\n" \
    "s_mov_b32 s50, 0x100000\n" \
    "1:\n" \
    "s_waitcnt vmcnt(0)\n" \
    "v_cmp_ne_u64 vcc, %[tag], v[34:35]\n" \
    "s_sub_u32 s50, s50, 1\n" \
    "s_cbranch_vccz 2f\n" \
    "s_cbranch_scc1 2f\n" \
    "global_load_dwordx2 v[34:35], v14, s[54:55] sc1\n" \
    "s_branch 1b\n" \
    "2:\n" \
    "s_waitcnt vmcnt(0)\n" \
    "s_barrier\n" \
    "global_load_dwordx4 v[44:47], v6, s[60:61] sc1\n" \
    "s_add_u32 s60, s60, 4096\n" \
    "s_addc_u32 s61, s61, 0\n" \
    "global_load_dwordx4 v[48:51], v6, s[60:61] sc1\n" \
    "s_add_u32 s60, s60, 4096\n" \
    "s_addc_u32 s61, s61, 0\n" \
    "global_load_dwordx4 v[52:55], v6, s[60:61] sc1\n" \
    "s_add_u32 s60, s60, 4096\n" \
    "s_addc_u32 s61, s61, 0\n" \
    "global_load_dwordx4 v[56:59], v6, s[60:61] sc1\n" \
    "s_add_u32 s60, s60, 4096\n" \
    "s_addc_u32 s61, s61, 0\n" \
    "global_load_dword v18, v15, s[56:57] sc1\n" \
    "v_lshrrev_b32 v17, 8, v92\n" \
    "v_lshl_add_u32 v19, v17, 4, v16\n" \
    "v_lshl_add_u32 v17, v17, 3, v16\n" \
    "v_mov_b32 v16, v19\n" \
    "v_add_u32 v8, 57344, v8\n" \
    "v_add_u32 v9, 57344, v9\n" \
    ".Lis6: s_cbranch_execz .Lis_ret\n" \
    "s_waitcnt vmcnt(4) lgkmcnt(10)\n" \
    "global_load_dwordx4 v[60:63], v6, s[60:61] sc1\n" \
    "s_add_u32 s60, s60, 4096\n" \
    "s_addc_u32 s61, s61, 0\n" \
    "v_mfma_f32_16x16x32_bf16 v[192:195], v[160:163], v[44:47], 0\n" \
    "v_mfma_f32_16x16x32_bf16 v[196:199], v[164:167], v[44:47], 0\n" \
    "ds_read_b128 v[160:163], v7 offset:20480\n" \
    "v_mfma_f32_16x16x32_bf16 v[200:203], v[168:171], v[44:47], 0\n" \
    "ds_read_b128 v[164:167], v7 offset:21504\n" \
    "v_mfma_f32_16x16x32_bf16 v[204:207], v[172:175], v[44:47], 0\n" \
    "ds_read_b128 v[168:171], v7 offset:22528\n" \
    "v_mfma_f32_16x16x32_bf16 v[208:211], v[176:179], v[44:47], 0\n" \
    "ds_read_b128 v[172:175], v7 offset:23552\n" \
    "ds_read_b128 v[176:179], v7 offset:24576\n" \
    "s_waitcnt vmcnt(4) lgkmcnt(10)\n" \
    "global_load_dwordx4 v[64:67], v6, s[60:61] sc1\n" \
    "s_add_u32 s60, s60, 4096\n" \
    "s_addc_u32 s61, s61, 0\n" \
    "v_mfma_f32_16x16x32_bf16 v[192:195], v[180:183], v[48:51], v[192:195]\n" \
    "v_mfma_f32_16x16x32_bf16 v[196:199], v[184:187], v[48:51], v[196:199]\n" \
    "ds_read_b128 v[180:183], v13 offset:0\n" \
    "v_mfma_f32_16x16x32_bf16 v[200:203], v[188:191], v[48:51], v[200:203]\n" \
    "ds_read_b128 v[184:187], v13 offset:1024\n" \
    "v_mfma_f32_16x16x32_bf16 v[204:207], v[96:99], v[48:51], v[204:207]\n" \
    "ds_read_b128 v[188:191], v13 offset:2048\n" \
    "v_mfma_f32_16x16x32_bf16 v[208:211], v[100:103], v[48:51], v[208:211]\n" \
    "ds_read_b128 v[96:99], v13 offset:3072\n" \
    "ds_read_b128 v[100:103], v13 offset:4096\n" \
    "s_waitcnt vmcnt(4) lgkmcnt(10)\n" \
    "global_load_dwordx4 v[68:71], v6, s[60:61] sc1\n" \
    "s_add_u32 s60, s60, 4096\n" \
    "s_addc_u32 s61, s61, 0\n" \
    "v_mfma_f32_16x16x32_bf16 v[192:195], v[104:107], v[52:55], v[192:195]\n" \
    "v_mfma_f32_16x16x32_bf16 v[196:199], v[108:111], v[52:55], v[196:199]\n" \
    "ds_read_b128 v[104:107], v13 offset:5120\n" \
    "v_mfma_f32_16x16x32_bf16 v[200:203], v[112:115], v[52:55], v[200:203]\n" \
    "ds_read_b128 v[108:111], v13 offset:6144\n" \
    "v_mfma_f32_16x16x32_bf16 v[204:207], v[116:119], v[52:55], v[204:207]\n" \
    "ds_read_b128 v[112:115], v13 offset:7168\n" \
    "v_mfma_f32_16x16x32_bf16 v[208:211], v[120:123], v[52:55], v[208:211]\n" \
    "ds_read_b128 v[116:119], v13 offset:8192\n" \
    "ds_read_b128 v[120:123], v13 offset:9216\n" \
    "s_waitcnt vmcnt(4) lgkmcnt(10)\n" \
    "global_load_dwordx4 v[72:75], v6, s[60:61] sc1\n" \
    "s_add_u32 s60, s60, 4096\n" \
    "s_addc_u32 s61, s61, 0\n" \
    "v_mfma_f32_16x16x32_bf16 v[192:195], v[124:127], v[56:59], v[192:195]\n" \
    "v_mfma_f32_16x16x32_bf16 v[196:199], v[244:247], v[56:59], v[196:199]\n" \
    "ds_read_b128 v[124:127], v13 offset:10240\n" \
    "v_mfma_f32_16x16x32_bf16 v[200:203], v[128:131], v[56:59], v[200:203]\n" \
    "ds_read_b128 v[244:247], v13 offset:11264\n" \
    "v_mfma_f32_16x16x32_bf16 v[204:207], v[132:135], v[56:59], v[204:207]\n" \
    "ds_read_b128 v[128:131], v13 offset:12288\n" \
    "v_mfma_f32_16x16x32_bf16 v[208:211], v[136:139], v[56:59], v[208:211]\n" \
    "ds_read_b128 v[132:135], v13 offset:13312\n" \
    "ds_read_b128 v[136:139], v13 offset:14336\n" \
    "s_waitcnt vmcnt(3) lgkmcnt(10)\n" \
    "global_load_dwordx4 v[76:79], v6, s[60:61] sc1\n" \
    "s_add_u32 s60, s60, 4096\n" \
    "s_addc_u32 s61, s61, 0\n" \
    "v_mfma_f32_16x16x32_bf16 v[192:195], v[140:143], v[60:63], v[192:195]\n" \
    "v_mfma_f32_16x16x32_bf16 v[196:199], v[144:147], v[60:63], v[196:199]\n" \
    "ds_read_b128 v[140:143], v13 offset:15360\n" \
    "v_mfma_f32_16x16x32_bf16 v[200:203], v[148:151], v[60:63], v[200:203]\n" \
    "ds_read_b128 v[144:147], v13 offset:16384\n" \
    "v_mfma_f32_16x16x32_bf16 v[204:207], v[152:155], v[60:63], v[204:207]\n" \
    "ds_read_b128 v[148:151], v13 offset:17408\n" \
    "v_mfma_f32_16x16x32_bf16 v[208:211], v[156:159], v[60:63], v[208:211]\n" \
    "ds_read_b128 v[152:155], v13 offset:18432\n" \
    "ds_read_b128 v[156:159], v13 offset:19456\n" \
    "s_waitcnt vmcnt(3) lgkmcnt(10)\n" \
    "global_load_dwordx4 v[80:83], v6, s[60:61] sc1\n" \
    "s_add_u32 s60, s60, 4096\n" \
    "s_addc_u32 s61, s61, 0\n" \
    "v_mfma_f32_16x16x32_bf16 v[192:195], v[160:163], v[64:67], v[192:195]\n" \
    "v_mfma_f32_16x16x32_bf16 v[196:199], v[164:167], v[64:67], v[196:199]\n" \
    "v_mfma_f32_16x16x32_bf16 v[200:203], v[168:171], v[64:67], v[200:203]\n" \
    "v_mfma_f32_16x16x32_bf16 v[204:207], v[172:175], v[64:67], v[204:207]\n" \
    "v_mfma_f32_16x16x32_bf16 v[208:211], v[176:179], v[64:67], v[208:211]\n" \
    "v_mul_f32 v224, v18, v224\n" \
    "v_mul_f32 v225, v18, v225\n" \
    "v_mul_f32 v226, v18, v226\n" \
    "v_mul_f32 v227, v18, v227\n" \
    "v_sub_f32 v19, 1.0, v18\n" \
    ".Lis7: s_cbranch_execz .Lis_ret\n" \
    "s_waitcnt vmcnt(3) lgkmcnt(10)\n" \
    "v_mfma_f32_16x16x32_bf16 v[192:195], v[180:183], v[68:71], v[192:195]\n" \
    "v_mfma_f32_16x16x32_bf16 v[196:199], v[184:187], v[68:71], v[196:199]\n" \
    "v_mfma_f32_16x16x32_bf16 v[200:203], v[188:191], v[68:71], v[200:203]\n" \
    "v_mfma_f32_16x16x32_bf16 v[204:207], v[96:99], v[68:71], v[204:207]\n" \
    "v_mfma_f32_16x16x32_bf16 v[208:211], v[100:103], v[68:71], v[208:211]\n" \
    "v_mul_f32 v228, v18, v228\n" \
    "v_mul_f32 v229, v18, v229\n" \
    "v_mul_f32 v230, v18, v230\n" \
    "v_mul_f32 v231, v18, v231\n" \
    "s_waitcnt vmcnt(2) lgkmcnt(10)\n" \
    "v_mfma_f32_16x16x32_bf16 v[192:195], v[104:107], v[72:75], v[192:195]\n" \
    "v_mfma_f32_16x16x32_bf16 v[196:199], v[108:111], v[72:75], v[196:199]\n" \
    "v_mfma_f32_16x16x32_bf16 v[200:203], v[112:115], v[72:75], v[200:203]\n" \
    "v_mfma_f32_16x16x32_bf16 v[204:207], v[116:119], v[72:75], v[204:207]\n" \
    "v_mfma_f32_16x16x32_bf16 v[208:211], v[120:123], v[72:75], v[208:211]\n" \
    "v_mul_f32 v232, v18, v232\n" \
    "v_mul_f32 v233, v18, v233\n" \
    "v_mul_f32 v234, v18, v234\n" \
    "v_mul_f32 v235, v18, v235\n" \
    "s_waitcnt vmcnt(1) lgkmcnt(5)\n" \
    "v_mfma_f32_16x16x32_bf16 v[192:195], v[124:127], v[76:79], v[192:195]\n" \
    "v_mfma_f32_16x16x32_bf16 v[196:199], v[244:247], v[76:79], v[196:199]\n" \
    "v_mfma_f32_16x16x32_bf16 v[200:203], v[128:131], v[76:79], v[200:203]\n" \
    "v_mfma_f32_16x16x32_bf16 v[204:207], v[132:135], v[76:79], v[204:207]\n" \
    "v_mfma_f32_16x16x32_bf16 v[208:211], v[136:139], v[76:79], v[208:211]\n" \
    "v_mul_f32 v236, v18, v236\n" \
    "v_mul_f32 v237, v18, v237\n" \
    "v_mul_f32 v238, v18, v238\n" \
    "v_mul_f32 v239, v18, v239\n" \
    "s_waitcnt vmcnt(0) lgkmcnt(0)\n" \
    "v_mfma_f32_16x16x32_bf16 v[192:195], v[140:143], v[80:83], v[192:195]\n" \
    "v_mfma_f32_16x16x32_bf16 v[196:199], v[144:147], v[80:83], v[196:199]\n" \
    "v_mfma_f32_16x16x32_bf16 v[200:203], v[148:151], v[80:83], v[200:203]\n" \
    "v_mfma_f32_16x16x32_bf16 v[204:207], v[152:155], v[80:83], v[204:207]\n" \
    "v_mfma_f32_16x16x32_bf16 v[208:211], v[156:159], v[80:83], v[208:211]\n" \
    "v_mul_f32 v240, v18, v240\n" \
    "v_mul_f32 v241, v18, v241\n" \
    "v_mul_f32 v242, v18, v242\n" \
    "v_mul_f32 v243, v18, v243\n" \
    "s_cmp_lt_u32 %[wid], 4\n" \
    "s_nop 2\n" \
    "s_cbranch_scc1 3f\n" \
    "v_fmac_f32 v224, v19, v192\n" \
    "v_fmac_f32 v225, v19, v193\n" \
    "v_fmac_f32 v226, v19, v194\n" \
    "v_fmac_f32 v227, v19, v195\n" \
    "ds_write_b128 v8, v[224:227] offset:0\n" \
    "v_fmac_f32 v228, v19, v196\n" \
    "v_fmac_f32 v229, v19, v197\n" \
    "v_fmac_f32 v230, v19, v198\n" \
    "v_fmac_f32 v231, v19, v199\n" \
    "ds_write_b128 v8, v[228:231] offset:1024\n" \
    "v_fmac_f32 v240, v19, v208\n" \
    "v_fmac_f32 v241, v19, v209\n" \
    "v_fmac_f32 v242, v19, v210\n" \
    "v_fmac_f32 v243, v19, v211\n" \
    "ds_write_b128 v8, v[240:243] offset:2048\n" \
    "v_fmac_f32 v232, v19, v200\n" \
    "v_fmac_f32 v233, v19, v201\n" \
    "v_fmac_f32 v234, v19, v202\n" \
    "v_fmac_f32 v235, v19, v203\n" \
    "v_fmac_f32 v236, v19, v204\n" \
    "v_fmac_f32 v237, v19, v205\n" \
    "v_fmac_f32 v238, v19, v206\n" \
    "v_fmac_f32 v239, v19, v207\n" \
    "s_waitcnt lgkmcnt(0)\n" \
    "s_barrier\n" \
    "ds_read_b128 v[20:23], v9 offset:0\n" \
    "ds_read_b128 v[24:27], v9 offset:1024\n" \
    "s_waitcnt lgkmcnt(1)\n" \
    "v_add_f32 v20, v20, v232\n" \
    "v_add_f32 v21, v21, v233\n" \
    "v_add_f32 v22, v22, v234\n" \
    "v_add_f32 v23, v23, v235\n" \
    "v_cvt_pk_bf16_f32 v192, v20, v21\n" \
    "v_cvt_pk_bf16_f32 v193, v22, v23\n" \
    "s_waitcnt lgkmcnt(0)\n" \
    "v_add_f32 v24, v24, v236\n" \
    "v_add_f32 v25, v25, v237\n" \
    "v_add_f32 v26, v26, v238\n" \
    "v_add_f32 v27, v27, v239\n" \
    "v_cvt_pk_bf16_f32 v194, v24, v25\n" \
    "v_cvt_pk_bf16_f32 v195, v26, v27\n" \
    "global_store_dwordx4 v16, v[192:195], s[64:65] offset:64\n" \
    "s_branch 4f\n" \
    "3:\n" \
    "v_fmac_f32 v232, v19, v200\n" \
    "v_fmac_f32 v233, v19, v201\n" \
    "v_fmac_f32 v234, v19, v202\n" \
    "v_fmac_f32 v235, v19, v203\n" \
    "ds_write_b128 v8, v[232:235] offset:0\n" \
    "v_fmac_f32 v236, v19, v204\n" \
    "v_fmac_f32 v237, v19, v205\n" \
    "v_fmac_f32 v238, v19, v206\n" \
    "v_fmac_f32 v239, v19, v207\n" \
    "ds_write_b128 v8, v[236:239] offset:1024\n" \
    "v_fmac_f32 v224, v19, v192\n" \
    "v_fmac_f32 v225, v19, v193\n" \
    "v_fmac_f32 v226, v19, v194\n" \
    "v_fmac_f32 v227, v19, v195\n" \
    "v_fmac_f32 v228, v19, v196\n" \
    "v_fmac_f32 v229, v19, v197\n" \
    "v_fmac_f32 v230, v19, v198\n" \
    "v_fmac_f32 v231, v19, v199\n" \
    "v_fmac_f32 v240, v19, v208\n" \
    "v_fmac_f32 v241, v19, v209\n" \
    "v_fmac_f32 v242, v19, v210\n" \
    "v_fmac_f32 v243, v19, v211\n" \
    "s_waitcnt lgkmcnt(0)\n" \
    "s_barrier\n" \
    "ds_read_b128 v[20:23], v9 offset:0\n" \
    "ds_read_b128 v[24:27], v9 offset:1024\n" \
    "ds_read_b128 v[28:31], v9 offset:2048\n" \
    "s_waitcnt lgkmcnt(2)\n" \
    "v_add_f32 v20, v20, v224\n" \
    "v_add_f32 v21, v21, v225\n" \
    "v_add_f32 v22, v22, v226\n" \
    "v_add_f32 v23, v23, v227\n" \
    "v_cvt_pk_bf16_f32 v192, v20, v21\n" \
    "v_cvt_pk_bf16_f32 v193, v22, v23\n" \
    "s_waitcnt lgkmcnt(1)\n" \
    "v_add_f32 v24, v24, v228\n" \
    "v_add_f32 v25, v25, v229\n" \
    "v_add_f32 v26, v26, v230\n" \
    "v_add_f32 v27, v27, v231\n" \
    "v_cvt_pk_bf16_f32 v194, v24, v25\n" \
    "v_cvt_pk_bf16_f32 v195, v26, v27\n" \
    "global_store_dwordx4 v16, v[192:195], s[64:65] offset:0\n" \
    "s_waitcnt lgkmcnt(0)\n" \
    "v_add_f32 v28, v28, v240\n" \
    "v_add_f32 v29, v29, v241\n" \
    "v_add_f32 v30, v30, v242\n" \
    "v_add_f32 v31, v31, v243\n" \
    "v_cvt_pk_bf16_f32 v196, v28, v29\n" \
    "v_cvt_pk_bf16_f32 v197, v30, v31\n" \
    "global_store_dwordx2 v17, v[196:197], s[64:65] offset:128\n" \
    "4:\n"
#define LOC_WIN \
    "v_mov_b32 v34, 0xff61b1e6\n" \
    "s_mov_b32 vcc_lo, 0x0\n" \
    "s_mov_b32 vcc_hi, 0x5\n" \
    "v_cndmask_b32_e64 v8, v34, 0, vcc\n" \
    "s_mov_b32 vcc_lo, 0x0\n" \
    "s_mov_b32 vcc_hi, 0xa\n" \
    "v_cndmask_b32_e64 v9, v34, 0, vcc\n" \
    "s_mov_b32 vcc_lo, 0x1\n" \
    "s_mov_b32 vcc_hi, 0x15\n" \
    "v_cndmask_b32_e64 v10, v34, 0, vcc\n" \
    "s_mov_b32 vcc_lo, 0x2\n" \
    "s_mov_b32 vcc_hi, 0x2a\n" \
    "v_cndmask_b32_e64 v11, v34, 0, vcc\n" \
    "s_mov_b32 vcc_lo, 0x55\n" \
    "s_mov_b32 vcc_hi, 0x555\n" \
    "v_cndmask_b32_e64 v12, v34, 0, vcc\n" \
    "s_mov_b32 vcc_lo, 0xaa\n" \
    "s_mov_b32 vcc_hi, 0xaaa\n" \
    "v_cndmask_b32_e64 v13, v34, 0, vcc\n" \
    "s_mov_b32 vcc_lo, 0x155\n" \
    "s_mov_b32 vcc_hi, 0x1555\n" \
    "v_cndmask_b32_e64 v14, v34, 0, vcc\n" \
    "s_mov_b32 vcc_lo, 0x2aa\n" \
    "s_mov_b32 vcc_hi, 0x2aaa\n" \
    "v_cndmask_b32_e64 v15, v34, 0, vcc\n" \
    "s_mov_b32 vcc_lo, 0x5555\n" \
    "s_mov_b32 vcc_hi, 0x55550\n" \
    "v_cndmask_b32_e64 v16, v34, 0, vcc\n" \
    "s_mov_b32 vcc_lo, 0xaaaa\n" \
    "s_mov_b32 vcc_hi, 0xaaaa0\n" \
    "v_cndmask_b32_e64 v17, v34, 0, vcc\n" \
    "s_mov_b32 vcc_lo, 0x15554\n" \
    "s_mov_b32 vcc_hi, 0x155540\n" \
    "v_cndmask_b32_e64 v18, v34, 0, vcc\n" \
    "s_mov_b32 vcc_lo, 0x2aaa8\n" \
    "s_mov_b32 vcc_hi, 0x2aaa80\n" \
    "v_cndmask_b32_e64 v19, v34, 0, vcc\n" \
    "s_mov_b32 vcc_lo, 0x555500\n" \
    "s_mov_b32 vcc_hi, 0x5555000\n" \
    "v_cndmask_b32_e64 v20, v34, 0, vcc\n" \
    "s_mov_b32 vcc_lo, 0xaaaa00\n" \
    "s_mov_b32 vcc_hi, 0xaaaa000\n" \
    "v_cndmask_b32_e64 v21, v34, 0, vcc\n" \
    "s_mov_b32 vcc_lo, 0x1555400\n" \
    "s_mov_b32 vcc_hi, 0x15554000\n" \
    "v_cndmask_b32_e64 v22, v34, 0, vcc\n" \
    "s_mov_b32 vcc_lo, 0x2aaa800\n" \
    "s_mov_b32 vcc_hi, 0x2aaa8000\n" \
    "v_cndmask_b32_e64 v23, v34, 0, vcc\n" \
    "s_mov_b32 vcc_lo, 0x55550000\n" \
    "s_mov_b32 vcc_hi, 0x55500000\n" \
    "v_cndmask_b32_e64 v24, v34, 0, vcc\n" \
    "s_mov_b32 vcc_lo, 0xaaaa0000\n" \
    "s_mov_b32 vcc_hi, 0xaaa00000\n" \
    "v_cndmask_b32_e64 v25, v34, 0, vcc\n" \
    "s_mov_b32 vcc_lo, 0x55540000\n" \
    "s_mov_b32 vcc_hi, 0x55400000\n" \
    "v_cndmask_b32_e64 v26, v34, 0, vcc\n" \
    "s_mov_b32 vcc_lo, 0xaaa80000\n" \
    "s_mov_b32 vcc_hi, 0xaa800000\n" \
    "v_cndmask_b32_e64 v27, v34, 0, vcc\n" \
    "s_mov_b32 vcc_lo, 0x55000000\n" \
    "s_mov_b32 vcc_hi, 0x50000000\n" \
    "v_cndmask_b32_e64 v28, v34, 0, vcc\n" \
    "s_mov_b32 vcc_lo, 0xaa000000\n" \
    "s_mov_b32 vcc_hi, 0xa0000000\n" \
    "v_cndmask_b32_e64 v29, v34, 0, vcc\n" \
    "s_mov_b32 vcc_lo, 0x54000000\n" \
    "s_mov_b32 vcc_hi, 0x40000000\n" \
    "v_cndmask_b32_e64 v30, v34, 0, vcc\n" \
    "s_mov_b32 vcc_lo, 0xa8000000\n" \
    "s_mov_b32 vcc_hi, 0x80000000\n" \
    "v_cndmask_b32_e64 v31, v34, 0, vcc\n"
#define GEMM_PROG \
    "s_mov_b32 s83, 0x8000\n" \
    "s_cmp_eq_u32 %[grp], 0\n" \
    "s_cbranch_scc1 70f\n" \
    "s_call_b64 s[76:77], .Lld_wx\n" \
    "s_waitcnt vmcnt(8) lgkmcnt(15)\n" \
    "s_barrier\n" \
    "s_call_b64 s[76:77], .Lld_wx\n" \
    "ds_read_b128 v[192:195], v120 offset:0\n" \
    "ds_read_b128 v[196:199], v122 offset:0\n" \
    "ds_read_b128 v[200:203], v120 offset:4096\n" \
    "ds_read_b128 v[204:207], v122 offset:4096\n" \
    "ds_read_b128 v[224:227], v121 offset:0\n" \
    "ds_read_b128 v[228:231], v123 offset:0\n" \
    "ds_read_b128 v[232:235], v121 offset:4096\n" \
    "ds_read_b128 v[236:239], v123 offset:4096\n" \
    "s_waitcnt vmcnt(8) lgkmcnt(15)\n" \
    "s_barrier\n" \
    "s_call_b64 s[76:77], .Lld_wx\n" \
    "ds_read_b128 v[0:3], v120 offset:16384\n" \
    "ds_read_b128 v[4:7], v122 offset:16384\n" \
    "ds_read_b128 v[8:11], v120 offset:20480\n" \
    "ds_read_b128 v[12:15], v122 offset:20480\n" \
    "ds_read_b128 v[16:19], v121 offset:16384\n" \
    "ds_read_b128 v[20:23], v123 offset:16384\n" \
    "ds_read_b128 v[24:27], v121 offset:20480\n" \
    "ds_read_b128 v[28:31], v123 offset:20480\n" \
    "s_waitcnt vmcnt(8) lgkmcnt(8)\n" \
    "s_barrier\n" \
    "s_call_b64 s[76:77], .Lld_wx\n" \
    "ds_read_b128 v[44:47], v120 offset:32768\n" \
    "ds_read_b128 v[48:51], v122 offset:32768\n" \
    "ds_read_b128 v[52:55], v120 offset:36864\n" \
    "ds_read_b128 v[56:59], v122 offset:36864\n" \
    "ds_read_b128 v[60:63], v121 offset:32768\n" \
    "ds_read_b128 v[64:67], v123 offset:32768\n" \
    "ds_read_b128 v[68:71], v121 offset:36864\n" \
    "ds_read_b128 v[72:75], v123 offset:36864\n" \
    "s_waitcnt vmcnt(8) lgkmcnt(8)\n" \
    "s_barrier\n" \
    "s_call_b64 s[76:77], .Lld_wx\n" \
    "ds_read_b128 v[96:99], v120 offset:49152\n" \
    "ds_read_b128 v[100:103], v122 offset:49152\n" \
    "ds_read_b128 v[104:107], v120 offset:53248\n" \
    "ds_read_b128 v[108:111], v122 offset:53248\n" \
    "ds_read_b128 v[32:35], v121 offset:49152\n" \
    "ds_read_b128 v[36:39], v123 offset:49152\n" \
    "ds_read_b128 v[76:79], v121 offset:53248\n" \
    "ds_read_b128 v[80:83], v123 offset:53248\n" \
    "s_waitcnt vmcnt(8) lgkmcnt(8)\n" \
    "s_barrier\n" \
    "s_call_b64 s[76:77], .Lld_wx\n" \
    "s_waitcnt vmcnt(8) lgkmcnt(0)\n" \
    "s_barrier\n" \
    "s_call_b64 s[76:77], .Lld_wx\n" \
    "s_waitcnt vmcnt(8) lgkmcnt(0)\n" \
    "s_barrier\n" \
    "s_call_b64 s[76:77], .Lld_wx\n" \
    "s_waitcnt vmcnt(8) lgkmcnt(0)\n" \
    "s_barrier\n" \
    "s_call_b64 s[76:77], .Lld_wx\n" \
    "s_mov_b64 s[80:81], %[wa]\n" \
    "s_call_b64 s[76:77], .Lld_set\n" \
    "s_call_b64 s[76:77], .Lld_w\n" \
    "s_waitcnt vmcnt(12)\n" \
    "s_barrier\n" \
    "s_call_b64 s[76:77], .Lld_w\n" \
    "s_add_u32 s82, s82, 48\n" \
    "s_call_b64 s[76:77], .Lhop\n" \
    "s_waitcnt vmcnt(8)\n" \
    "s_barrier\n" \
    "s_call_b64 s[76:77], .Lld_w\n" \
    "s_call_b64 s[76:77], .Lhop\n" \
    "v_add_u32 v120, 0x10000, v120\n" \
    "v_add_u32 v121, 0x10000, v121\n" \
    "v_add_u32 v122, 0x10000, v122\n" \
    "v_add_u32 v123, 0x10000, v123\n" \
    "s_mov_b32 s58, 2\n" \
    "s_mov_b32 s59, 2\n" \
    "s_mov_b32 s61, 0xffff8000\n" \
    "s_mov_b32 s84, 0\n" \
    "s_setprio 3\n" \
    "s_cmp_lg_u32 %[isv], 0\n" \
    "s_waitcnt vmcnt(8)\n" \
    "s_barrier\n" \
    "ds_read_b64_tr_b16 v[208:209], v124 offset:0\n" \
    "ds_read_b64_tr_b16 v[210:211], v124 offset:1024\n" \
    "ds_read_b64_tr_b16 v[212:213], v125 offset:0\n" \
    "ds_read_b64_tr_b16 v[214:215], v125 offset:1024\n" \
    "ds_read_b64_tr_b16 v[216:217], v126 offset:0\n" \
    "ds_read_b64_tr_b16 v[218:219], v126 offset:1024\n" \
    "ds_read_b64_tr_b16 v[220:221], v127 offset:0\n" \
    "ds_read_b64_tr_b16 v[222:223], v127 offset:1024\n" \
    "s_cbranch_scc1 95f\n" \
    "s_branch 85f\n" \
    "70:\n" \
    "s_mov_b32 s58, 0\n" \
    "s_mov_b32 s59, 0\n" \
    "s_mov_b32 s61, 0x8000\n" \
    "s_mov_b32 s84, 5\n" \
    "s_lshl_b32 s75, %[isv], 4\n" \
    "s_add_u32 s82, s82, s75\n" \
    "s_call_b64 s[76:77], .Lhop\n" \
    "s_and_b32 s82, s82, 15\n" \
    "s_add_u32 s82, s82, 32\n" \
    "s_call_b64 s[76:77], .Lhop\n" \
    "s_cmp_lg_u32 %[isv], 0\n" \
    "s_setprio 3\n" \
    "s_waitcnt lgkmcnt(0)\n" \
    "s_barrier\n" \
    "ds_read_b64_tr_b16 v[208:209], v124 offset:0\n" \
    "ds_read_b64_tr_b16 v[210:211], v124 offset:1024\n" \
    "ds_read_b128 v[192:195], v120 offset:0\n" \
    "ds_read_b128 v[196:199], v122 offset:0\n" \
    "ds_read_b128 v[200:203], v120 offset:4096\n" \
    "ds_read_b128 v[204:207], v122 offset:4096\n" \
    "ds_read_b64_tr_b16 v[212:213], v125 offset:0\n" \
    "ds_read_b64_tr_b16 v[214:215], v125 offset:1024\n" \
    "ds_read_b64_tr_b16 v[216:217], v126 offset:0\n" \
    "ds_read_b64_tr_b16 v[218:219], v126 offset:1024\n" \
    "ds_read_b64_tr_b16 v[220:221], v127 offset:0\n" \
    "ds_read_b64_tr_b16 v[222:223], v127 offset:1024\n" \
    "s_cbranch_scc1 90f\n" \
    "s_branch 80f\n" \
    ".p2align 12\n" \
    ".Lis8: s_cbranch_execz .Lis_ret\n" \
    "85:\n" \
    "s_waitcnt lgkmcnt(0)\n" \
    "v_mfma_f32_16x16x32_bf16 v[128:131], v[208:211], v[192:195], v[128:131]\n" \
    "ds_read_b64_tr_b16 v[240:241], v124 offset:8192\n" \
    "v_mfma_f32_16x16x32_bf16 v[132:135], v[208:211], v[196:199], v[132:135]\n" \
    "ds_read_b64_tr_b16 v[242:243], v124 offset:9216\n" \
    "v_mfma_f32_16x16x32_bf16 v[136:139], v[208:211], v[200:203], v[136:139]\n" \
    "ds_read_b64_tr_b16 v[244:245], v125 offset:8192\n" \
    "v_mfma_f32_16x16x32_bf16 v[140:143], v[208:211], v[204:207], v[140:143]\n" \
    "ds_read_b64_tr_b16 v[246:247], v125 offset:9216\n" \
    "v_mfma_f32_16x16x32_bf16 v[144:147], v[212:215], v[192:195], v[144:147]\n" \
    "ds_read_b64_tr_b16 v[248:249], v126 offset:8192\n" \
    "v_mfma_f32_16x16x32_bf16 v[148:151], v[212:215], v[196:199], v[148:151]\n" \
    "ds_read_b64_tr_b16 v[250:251], v126 offset:9216\n" \
    "v_mfma_f32_16x16x32_bf16 v[152:155], v[212:215], v[200:203], v[152:155]\n" \
    "ds_read_b64_tr_b16 v[252:253], v127 offset:8192\n" \
    "v_mfma_f32_16x16x32_bf16 v[156:159], v[212:215], v[204:207], v[156:159]\n" \
    "ds_read_b64_tr_b16 v[254:255], v127 offset:9216\n" \
    "v_mfma_f32_16x16x32_bf16 v[160:163], v[216:219], v[192:195], v[160:163]\n" \
    "v_mfma_f32_16x16x32_bf16 v[164:167], v[216:219], v[196:199], v[164:167]\n" \
    "v_mfma_f32_16x16x32_bf16 v[168:171], v[216:219], v[200:203], v[168:171]\n" \
    "v_mfma_f32_16x16x32_bf16 v[172:175], v[216:219], v[204:207], v[172:175]\n" \
    "v_mfma_f32_16x16x32_bf16 v[176:179], v[220:223], v[192:195], v[176:179]\n" \
    "v_mfma_f32_16x16x32_bf16 v[180:183], v[220:223], v[196:199], v[180:183]\n" \
    "v_mfma_f32_16x16x32_bf16 v[184:187], v[220:223], v[200:203], v[184:187]\n" \
    "v_mfma_f32_16x16x32_bf16 v[188:191], v[220:223], v[204:207], v[188:191]\n" \
    "s_waitcnt vmcnt(0) lgkmcnt(0)\n" \
    "s_barrier\n" \
    "v_mfma_f32_16x16x32_bf16 v[128:131], v[240:243], v[224:227], v[128:131]\n" \
    "ds_read_b64_tr_b16 v[208:209], v124 offset:16384\n" \
    "v_mfma_f32_16x16x32_bf16 v[132:135], v[240:243], v[228:231], v[132:135]\n" \
    "ds_read_b64_tr_b16 v[210:211], v124 offset:17408\n" \
    "v_mfma_f32_16x16x32_bf16 v[136:139], v[240:243], v[232:235], v[136:139]\n" \
    "ds_read_b64_tr_b16 v[212:213], v125 offset:16384\n" \
    "v_mfma_f32_16x16x32_bf16 v[140:143], v[240:243], v[236:239], v[140:143]\n" \
    "ds_read_b64_tr_b16 v[214:215], v125 offset:17408\n" \
    "v_mfma_f32_16x16x32_bf16 v[144:147], v[244:247], v[224:227], v[144:147]\n" \
    "ds_read_b64_tr_b16 v[216:217], v126 offset:16384\n" \
    "v_mfma_f32_16x16x32_bf16 v[148:151], v[244:247], v[228:231], v[148:151]\n" \
    "ds_read_b64_tr_b16 v[218:219], v126 offset:17408\n" \
    "v_mfma_f32_16x16x32_bf16 v[152:155], v[244:247], v[232:235], v[152:155]\n" \
    "ds_read_b64_tr_b16 v[220:221], v127 offset:16384\n" \
    "v_mfma_f32_16x16x32_bf16 v[156:159], v[244:247], v[236:239], v[156:159]\n" \
    "ds_read_b64_tr_b16 v[222:223], v127 offset:17408\n" \
    "v_mfma_f32_16x16x32_bf16 v[160:163], v[248:251], v[224:227], v[160:163]\n" \
    "v_mfma_f32_16x16x32_bf16 v[164:167], v[248:251], v[228:231], v[164:167]\n" \
    "v_mfma_f32_16x16x32_bf16 v[168:171], v[248:251], v[232:235], v[168:171]\n" \
    "v_mfma_f32_16x16x32_bf16 v[172:175], v[248:251], v[236:239], v[172:175]\n" \
    "v_mfma_f32_16x16x32_bf16 v[176:179], v[252:255], v[224:227], v[176:179]\n" \
    "v_mfma_f32_16x16x32_bf16 v[180:183], v[252:255], v[228:231], v[180:183]\n" \
    "v_mfma_f32_16x16x32_bf16 v[184:187], v[252:255], v[232:235], v[184:187]\n" \
    "v_mfma_f32_16x16x32_bf16 v[188:191], v[252:255], v[236:239], v[188:191]\n" \
    "s_waitcnt lgkmcnt(0)\n" \
    "v_mfma_f32_16x16x32_bf16 v[128:131], v[208:211], v[0:3], v[128:131]\n" \
    "ds_read_b64_tr_b16 v[240:241], v124 offset:24576\n" \
    "v_mfma_f32_16x16x32_bf16 v[132:135], v[208:211], v[4:7], v[132:135]\n" \
    "ds_read_b64_tr_b16 v[242:243], v124 offset:25600\n" \
    "v_mfma_f32_16x16x32_bf16 v[136:139], v[208:211], v[8:11], v[136:139]\n" \
    "ds_read_b64_tr_b16 v[244:245], v125 offset:24576\n" \
    "v_mfma_f32_16x16x32_bf16 v[140:143], v[208:211], v[12:15], v[140:143]\n" \
    "ds_read_b64_tr_b16 v[246:247], v125 offset:25600\n" \
    "v_mfma_f32_16x16x32_bf16 v[144:147], v[212:215], v[0:3], v[144:147]\n" \
    "ds_read_b64_tr_b16 v[248:249], v126 offset:24576\n" \
    "v_mfma_f32_16x16x32_bf16 v[148:151], v[212:215], v[4:7], v[148:151]\n" \
    "ds_read_b64_tr_b16 v[250:251], v126 offset:25600\n" \
    "v_mfma_f32_16x16x32_bf16 v[152:155], v[212:215], v[8:11], v[152:155]\n" \
    "ds_read_b64_tr_b16 v[252:253], v127 offset:24576\n" \
    "v_mfma_f32_16x16x32_bf16 v[156:159], v[212:215], v[12:15], v[156:159]\n" \
    "ds_read_b64_tr_b16 v[254:255], v127 offset:25600\n" \
    "v_mfma_f32_16x16x32_bf16 v[160:163], v[216:219], v[0:3], v[160:163]\n" \
    "v_mfma_f32_16x16x32_bf16 v[164:167], v[216:219], v[4:7], v[164:167]\n" \
    "v_mfma_f32_16x16x32_bf16 v[168:171], v[216:219], v[8:11], v[168:171]\n" \
    "v_mfma_f32_16x16x32_bf16 v[172:175], v[216:219], v[12:15], v[172:175]\n" \
    "v_mfma_f32_16x16x32_bf16 v[176:179], v[220:223], v[0:3], v[176:179]\n" \
    "v_add_u32 v124, s61, v124\n" \
    "v_add_u32 v125, s61, v125\n" \
    "v_mfma_f32_16x16x32_bf16 v[180:183], v[220:223], v[4:7], v[180:183]\n" \
    "v_add_u32 v126, s61, v126\n" \
    "v_add_u32 v127, s61, v127\n" \
    "v_mfma_f32_16x16x32_bf16 v[184:187], v[220:223], v[8:11], v[184:187]\n" \
    "v_mfma_f32_16x16x32_bf16 v[188:191], v[220:223], v[12:15], v[188:191]\n" \
    "s_waitcnt vmcnt(0) lgkmcnt(0)\n" \
    "v_mfma_f32_16x16x32_bf16 v[128:131], v[240:243], v[16:19], v[128:131]\n" \
    "ds_read_b64_tr_b16 v[208:209], v124 offset:0\n" \
    "v_mfma_f32_16x16x32_bf16 v[132:135], v[240:243], v[20:23], v[132:135]\n" \
    "ds_read_b64_tr_b16 v[210:211], v124 offset:1024\n" \
    "v_mfma_f32_16x16x32_bf16 v[136:139], v[240:243], v[24:27], v[136:139]\n" \
    "ds_read_b64_tr_b16 v[212:213], v125 offset:0\n" \
    "v_mfma_f32_16x16x32_bf16 v[140:143], v[240:243], v[28:31], v[140:143]\n" \
    "ds_read_b64_tr_b16 v[214:215], v125 offset:1024\n" \
    "v_mfma_f32_16x16x32_bf16 v[144:147], v[244:247], v[16:19], v[144:147]\n" \
    "ds_read_b64_tr_b16 v[216:217], v126 offset:0\n" \
    "v_mfma_f32_16x16x32_bf16 v[148:151], v[244:247], v[20:23], v[148:151]\n" \
    "ds_read_b64_tr_b16 v[218:219], v126 offset:1024\n" \
    "v_mfma_f32_16x16x32_bf16 v[152:155], v[244:247], v[24:27], v[152:155]\n" \
    "ds_read_b64_tr_b16 v[220:221], v127 offset:0\n" \
    "v_mfma_f32_16x16x32_bf16 v[156:159], v[244:247], v[28:31], v[156:159]\n" \
    "ds_read_b64_tr_b16 v[222:223], v127 offset:1024\n" \
    "v_mfma_f32_16x16x32_bf16 v[160:163], v[248:251], v[16:19], v[160:163]\n" \
    "v_mfma_f32_16x16x32_bf16 v[164:167], v[248:251], v[20:23], v[164:167]\n" \
    "v_mfma_f32_16x16x32_bf16 v[168:171], v[248:251], v[24:27], v[168:171]\n" \
    "v_mfma_f32_16x16x32_bf16 v[172:175], v[248:251], v[28:31], v[172:175]\n" \
    "v_mfma_f32_16x16x32_bf16 v[176:179], v[252:255], v[16:19], v[176:179]\n" \
    "s_sub_u32 s61, 0, s61\n" \
    "v_mfma_f32_16x16x32_bf16 v[180:183], v[252:255], v[20:23], v[180:183]\n" \
    "v_mfma_f32_16x16x32_bf16 v[184:187], v[252:255], v[24:27], v[184:187]\n" \
    "v_mfma_f32_16x16x32_bf16 v[188:191], v[252:255], v[28:31], v[188:191]\n" \
    "s_waitcnt lgkmcnt(0)\n" \
    "v_mfma_f32_16x16x32_bf16 v[128:131], v[208:211], v[44:47], v[128:131]\n" \
    "ds_read_b64_tr_b16 v[240:241], v124 offset:8192\n" \
    "v_mfma_f32_16x16x32_bf16 v[132:135], v[208:211], v[48:51], v[132:135]\n" \
    "ds_read_b64_tr_b16 v[242:243], v124 offset:9216\n" \
    "v_mfma_f32_16x16x32_bf16 v[136:139], v[208:211], v[52:55], v[136:139]\n" \
    "ds_read_b64_tr_b16 v[244:245], v125 offset:8192\n" \
    "v_mfma_f32_16x16x32_bf16 v[140:143], v[208:211], v[56:59], v[140:143]\n" \
    "ds_read_b64_tr_b16 v[246:247], v125 offset:9216\n" \
    "v_mfma_f32_16x16x32_bf16 v[144:147], v[212:215], v[44:47], v[144:147]\n" \
    "ds_read_b64_tr_b16 v[248:249], v126 offset:8192\n" \
    "v_mfma_f32_16x16x32_bf16 v[148:151], v[212:215], v[48:51], v[148:151]\n" \
    "ds_read_b64_tr_b16 v[250:251], v126 offset:9216\n" \
    "v_mfma_f32_16x16x32_bf16 v[152:155], v[212:215], v[52:55], v[152:155]\n" \
    "ds_read_b64_tr_b16 v[252:253], v127 offset:8192\n" \
    "v_mfma_f32_16x16x32_bf16 v[156:159], v[212:215], v[56:59], v[156:159]\n" \
    "ds_read_b64_tr_b16 v[254:255], v127 offset:9216\n" \
    "v_mfma_f32_16x16x32_bf16 v[160:163], v[216:219], v[44:47], v[160:163]\n" \
    "v_mfma_f32_16x16x32_bf16 v[164:167], v[216:219], v[48:51], v[164:167]\n" \
    "v_mfma_f32_16x16x32_bf16 v[168:171], v[216:219], v[52:55], v[168:171]\n" \
    "v_mfma_f32_16x16x32_bf16 v[172:175], v[216:219], v[56:59], v[172:175]\n" \
    "v_mfma_f32_16x16x32_bf16 v[176:179], v[220:223], v[44:47], v[176:179]\n" \
    "v_mfma_f32_16x16x32_bf16 v[180:183], v[220:223], v[48:51], v[180:183]\n" \
    "v_mfma_f32_16x16x32_bf16 v[184:187], v[220:223], v[52:55], v[184:187]\n" \
    "v_mfma_f32_16x16x32_bf16 v[188:191], v[220:223], v[56:59], v[188:191]\n" \
    "s_waitcnt vmcnt(0) lgkmcnt(0)\n" \
    "s_barrier\n" \
    "v_mfma_f32_16x16x32_bf16 v[128:131], v[240:243], v[60:63], v[128:131]\n" \
    "ds_read_b64_tr_b16 v[208:209], v124 offset:16384\n" \
    "v_mfma_f32_16x16x32_bf16 v[132:135], v[240:243], v[64:67], v[132:135]\n" \
    "ds_read_b64_tr_b16 v[210:211], v124 offset:17408\n" \
    "v_mfma_f32_16x16x32_bf16 v[136:139], v[240:243], v[68:71], v[136:139]\n" \
    "ds_read_b64_tr_b16 v[212:213], v125 offset:16384\n" \
    "v_mfma_f32_16x16x32_bf16 v[140:143], v[240:243], v[72:75], v[140:143]\n" \
    "ds_read_b64_tr_b16 v[214:215], v125 offset:17408\n" \
    "v_mfma_f32_16x16x32_bf16 v[144:147], v[244:247], v[60:63], v[144:147]\n" \
    "ds_read_b64_tr_b16 v[216:217], v126 offset:16384\n" \
    "v_mfma_f32_16x16x32_bf16 v[148:151], v[244:247], v[64:67], v[148:151]\n" \
    "ds_read_b64_tr_b16 v[218:219], v126 offset:17408\n" \
    "v_mfma_f32_16x16x32_bf16 v[152:155], v[244:247], v[68:71], v[152:155]\n" \
    "ds_read_b64_tr_b16 v[220:221], v127 offset:16384\n" \
    "v_mfma_f32_16x16x32_bf16 v[156:159], v[244:247], v[72:75], v[156:159]\n" \
    "ds_read_b64_tr_b16 v[222:223], v127 offset:17408\n" \
    "v_mfma_f32_16x16x32_bf16 v[160:163], v[248:251], v[60:63], v[160:163]\n" \
    "v_mfma_f32_16x16x32_bf16 v[164:167], v[248:251], v[64:67], v[164:167]\n" \
    "v_mfma_f32_16x16x32_bf16 v[168:171], v[248:251], v[68:71], v[168:171]\n" \
    "v_mfma_f32_16x16x32_bf16 v[172:175], v[248:251], v[72:75], v[172:175]\n" \
    "v_mfma_f32_16x16x32_bf16 v[176:179], v[252:255], v[60:63], v[176:179]\n" \
    "v_mfma_f32_16x16x32_bf16 v[180:183], v[252:255], v[64:67], v[180:183]\n" \
    "v_mfma_f32_16x16x32_bf16 v[184:187], v[252:255], v[68:71], v[184:187]\n" \
    ".Lis9: s_cbranch_execz .Lis_ret\n" \
    "v_mfma_f32_16x16x32_bf16 v[188:191], v[252:255], v[72:75], v[188:191]\n" \
    "s_waitcnt lgkmcnt(0)\n" \
    "v_mfma_f32_16x16x32_bf16 v[128:131], v[208:211], v[96:99], v[128:131]\n" \
    "ds_read_b64_tr_b16 v[240:241], v124 offset:24576\n" \
    "v_mfma_f32_16x16x32_bf16 v[132:135], v[208:211], v[100:103], v[132:135]\n" \
    "ds_read_b64_tr_b16 v[242:243], v124 offset:25600\n" \
    "v_mfma_f32_16x16x32_bf16 v[136:139], v[208:211], v[104:107], v[136:139]\n" \
    "ds_read_b64_tr_b16 v[244:245], v125 offset:24576\n" \
    "v_mfma_f32_16x16x32_bf16 v[140:143], v[208:211], v[108:111], v[140:143]\n" \
    "ds_read_b64_tr_b16 v[246:247], v125 offset:25600\n" \
    "v_mfma_f32_16x16x32_bf16 v[144:147], v[212:215], v[96:99], v[144:147]\n" \
    "ds_read_b64_tr_b16 v[248:249], v126 offset:24576\n" \
    "v_mfma_f32_16x16x32_bf16 v[148:151], v[212:215], v[100:103], v[148:151]\n" \
    "ds_read_b64_tr_b16 v[250:251], v126 offset:25600\n" \
    "v_mfma_f32_16x16x32_bf16 v[152:155], v[212:215], v[104:107], v[152:155]\n" \
    "ds_read_b64_tr_b16 v[252:253], v127 offset:24576\n" \
    "v_mfma_f32_16x16x32_bf16 v[156:159], v[212:215], v[108:111], v[156:159]\n" \
    "ds_read_b64_tr_b16 v[254:255], v127 offset:25600\n" \
    "v_mfma_f32_16x16x32_bf16 v[160:163], v[216:219], v[96:99], v[160:163]\n" \
    "v_mfma_f32_16x16x32_bf16 v[164:167], v[216:219], v[100:103], v[164:167]\n" \
    "v_mfma_f32_16x16x32_bf16 v[168:171], v[216:219], v[104:107], v[168:171]\n" \
    "v_mfma_f32_16x16x32_bf16 v[172:175], v[216:219], v[108:111], v[172:175]\n" \
    "v_mfma_f32_16x16x32_bf16 v[176:179], v[220:223], v[96:99], v[176:179]\n" \
    "v_add_u32 v124, s61, v124\n" \
    "v_add_u32 v125, s61, v125\n" \
    "v_mfma_f32_16x16x32_bf16 v[180:183], v[220:223], v[100:103], v[180:183]\n" \
    "v_add_u32 v126, s61, v126\n" \
    "v_add_u32 v127, s61, v127\n" \
    "v_mfma_f32_16x16x32_bf16 v[184:187], v[220:223], v[104:107], v[184:187]\n" \
    "v_mfma_f32_16x16x32_bf16 v[188:191], v[220:223], v[108:111], v[188:191]\n" \
    "s_waitcnt vmcnt(0) lgkmcnt(0)\n" \
    "v_mfma_f32_16x16x32_bf16 v[128:131], v[240:243], v[32:35], v[128:131]\n" \
    "ds_read_b64_tr_b16 v[208:209], v124 offset:0\n" \
    "v_mfma_f32_16x16x32_bf16 v[132:135], v[240:243], v[36:39], v[132:135]\n" \
    "ds_read_b64_tr_b16 v[210:211], v124 offset:1024\n" \
    "v_mfma_f32_16x16x32_bf16 v[136:139], v[240:243], v[76:79], v[136:139]\n" \
    "ds_read_b128 v[192:195], v120 offset:0\n" \
    "v_mfma_f32_16x16x32_bf16 v[140:143], v[240:243], v[80:83], v[140:143]\n" \
    "ds_read_b128 v[196:199], v122 offset:0\n" \
    "v_mfma_f32_16x16x32_bf16 v[144:147], v[244:247], v[32:35], v[144:147]\n" \
    "ds_read_b128 v[200:203], v120 offset:4096\n" \
    "v_mfma_f32_16x16x32_bf16 v[148:151], v[244:247], v[36:39], v[148:151]\n" \
    "ds_read_b128 v[204:207], v122 offset:4096\n" \
    "v_mfma_f32_16x16x32_bf16 v[152:155], v[244:247], v[76:79], v[152:155]\n" \
    "ds_read_b64_tr_b16 v[212:213], v125 offset:0\n" \
    "v_mfma_f32_16x16x32_bf16 v[156:159], v[244:247], v[80:83], v[156:159]\n" \
    "ds_read_b64_tr_b16 v[214:215], v125 offset:1024\n" \
    "v_mfma_f32_16x16x32_bf16 v[160:163], v[248:251], v[32:35], v[160:163]\n" \
    "ds_read_b64_tr_b16 v[216:217], v126 offset:0\n" \
    "v_mfma_f32_16x16x32_bf16 v[164:167], v[248:251], v[36:39], v[164:167]\n" \
    "ds_read_b64_tr_b16 v[218:219], v126 offset:1024\n" \
    "v_mfma_f32_16x16x32_bf16 v[168:171], v[248:251], v[76:79], v[168:171]\n" \
    "ds_read_b64_tr_b16 v[220:221], v127 offset:0\n" \
    "v_mfma_f32_16x16x32_bf16 v[172:175], v[248:251], v[80:83], v[172:175]\n" \
    "ds_read_b64_tr_b16 v[222:223], v127 offset:1024\n" \
    "v_mfma_f32_16x16x32_bf16 v[176:179], v[252:255], v[32:35], v[176:179]\n" \
    "s_sub_u32 s61, 0, s61\n" \
    "v_mfma_f32_16x16x32_bf16 v[180:183], v[252:255], v[36:39], v[180:183]\n" \
    "v_mfma_f32_16x16x32_bf16 v[184:187], v[252:255], v[76:79], v[184:187]\n" \
    "v_mfma_f32_16x16x32_bf16 v[188:191], v[252:255], v[80:83], v[188:191]\n" \
    ".Lis10: s_cbranch_execz .Lis_ret\n" \
    "80:\n" \
    "s_waitcnt lgkmcnt(0)\n" \
    "v_mfma_f32_16x16x32_bf16 v[128:131], v[208:211], v[192:195], v[128:131]\n" \
    "ds_read_b64_tr_b16 v[240:241], v124 offset:8192\n" \
    "v_mfma_f32_16x16x32_bf16 v[132:135], v[208:211], v[196:199], v[132:135]\n" \
    "ds_read_b64_tr_b16 v[242:243], v124 offset:9216\n" \
    "v_mfma_f32_16x16x32_bf16 v[136:139], v[208:211], v[200:203], v[136:139]\n" \
    "ds_read_b128 v[224:227], v121 offset:0\n" \
    "v_mfma_f32_16x16x32_bf16 v[140:143], v[208:211], v[204:207], v[140:143]\n" \
    "ds_read_b128 v[228:231], v123 offset:0\n" \
    "v_mfma_f32_16x16x32_bf16 v[144:147], v[212:215], v[192:195], v[144:147]\n" \
    "ds_read_b128 v[232:235], v121 offset:4096\n" \
    "v_mfma_f32_16x16x32_bf16 v[148:151], v[212:215], v[196:199], v[148:151]\n" \
    "ds_read_b128 v[236:239], v123 offset:4096\n" \
    "v_mfma_f32_16x16x32_bf16 v[152:155], v[212:215], v[200:203], v[152:155]\n" \
    "ds_read_b64_tr_b16 v[244:245], v125 offset:8192\n" \
    "v_mfma_f32_16x16x32_bf16 v[156:159], v[212:215], v[204:207], v[156:159]\n" \
    "ds_read_b64_tr_b16 v[246:247], v125 offset:9216\n" \
    "v_mfma_f32_16x16x32_bf16 v[160:163], v[216:219], v[192:195], v[160:163]\n" \
    "ds_read_b64_tr_b16 v[248:249], v126 offset:8192\n" \
    "v_mfma_f32_16x16x32_bf16 v[164:167], v[216:219], v[196:199], v[164:167]\n" \
    "ds_read_b64_tr_b16 v[250:251], v126 offset:9216\n" \
    "v_mfma_f32_16x16x32_bf16 v[168:171], v[216:219], v[200:203], v[168:171]\n" \
    "ds_read_b64_tr_b16 v[252:253], v127 offset:8192\n" \
    "v_mfma_f32_16x16x32_bf16 v[172:175], v[216:219], v[204:207], v[172:175]\n" \
    "ds_read_b64_tr_b16 v[254:255], v127 offset:9216\n" \
    "v_mfma_f32_16x16x32_bf16 v[176:179], v[220:223], v[192:195], v[176:179]\n" \
    "s_add_u32 s59, s59, 1\n" \
    "s_cmp_eq_u32 s59, 3\n" \
    "v_mfma_f32_16x16x32_bf16 v[180:183], v[220:223], v[196:199], v[180:183]\n" \
    "s_cselect_b32 s60, 0xffff0000, s83\n" \
    "s_cselect_b32 s59, 0, s59\n" \
    "v_mfma_f32_16x16x32_bf16 v[184:187], v[220:223], v[200:203], v[184:187]\n" \
    "s_cmp_lt_u32 s58, s84\n" \
    "v_mfma_f32_16x16x32_bf16 v[188:191], v[220:223], v[204:207], v[188:191]\n" \
    "s_waitcnt lgkmcnt(0)\n" \
    "s_barrier\n" \
    "v_mfma_f32_16x16x32_bf16 v[128:131], v[240:243], v[224:227], v[128:131]\n" \
    "ds_read_b64_tr_b16 v[208:209], v124 offset:16384\n" \
    "v_mfma_f32_16x16x32_bf16 v[132:135], v[240:243], v[228:231], v[132:135]\n" \
    "ds_read_b64_tr_b16 v[210:211], v124 offset:17408\n" \
    "v_mfma_f32_16x16x32_bf16 v[136:139], v[240:243], v[232:235], v[136:139]\n" \
    "ds_read_b128 v[192:195], v120 offset:16384\n" \
    "v_mfma_f32_16x16x32_bf16 v[140:143], v[240:243], v[236:239], v[140:143]\n" \
    "ds_read_b128 v[196:199], v122 offset:16384\n" \
    "v_mfma_f32_16x16x32_bf16 v[144:147], v[244:247], v[224:227], v[144:147]\n" \
    "ds_read_b128 v[200:203], v120 offset:20480\n" \
    "v_mfma_f32_16x16x32_bf16 v[148:151], v[244:247], v[228:231], v[148:151]\n" \
    "ds_read_b128 v[204:207], v122 offset:20480\n" \
    "v_mfma_f32_16x16x32_bf16 v[152:155], v[244:247], v[232:235], v[152:155]\n" \
    "ds_read_b64_tr_b16 v[212:213], v125 offset:16384\n" \
    "v_mfma_f32_16x16x32_bf16 v[156:159], v[244:247], v[236:239], v[156:159]\n" \
    "ds_read_b64_tr_b16 v[214:215], v125 offset:17408\n" \
    "v_mfma_f32_16x16x32_bf16 v[160:163], v[248:251], v[224:227], v[160:163]\n" \
    "ds_read_b64_tr_b16 v[216:217], v126 offset:16384\n" \
    "v_mfma_f32_16x16x32_bf16 v[164:167], v[248:251], v[228:231], v[164:167]\n" \
    "ds_read_b64_tr_b16 v[218:219], v126 offset:17408\n" \
    "v_mfma_f32_16x16x32_bf16 v[168:171], v[248:251], v[232:235], v[168:171]\n" \
    "ds_read_b64_tr_b16 v[220:221], v127 offset:16384\n" \
    "v_mfma_f32_16x16x32_bf16 v[172:175], v[248:251], v[236:239], v[172:175]\n" \
    "ds_read_b64_tr_b16 v[222:223], v127 offset:17408\n" \
    "v_mfma_f32_16x16x32_bf16 v[176:179], v[252:255], v[224:227], v[176:179]\n" \
    "v_add_u32 v120, s60, v120\n" \
    "v_mfma_f32_16x16x32_bf16 v[180:183], v[252:255], v[228:231], v[180:183]\n" \
    "v_add_u32 v122, s60, v122\n" \
    "v_mfma_f32_16x16x32_bf16 v[184:187], v[252:255], v[232:235], v[184:187]\n" \
    "v_mfma_f32_16x16x32_bf16 v[188:191], v[252:255], v[236:239], v[188:191]\n" \
    "s_waitcnt lgkmcnt(0)\n" \
    "v_mfma_f32_16x16x32_bf16 v[128:131], v[208:211], v[192:195], v[128:131]\n" \
    "ds_read_b64_tr_b16 v[240:241], v124 offset:24576\n" \
    "v_mfma_f32_16x16x32_bf16 v[132:135], v[208:211], v[196:199], v[132:135]\n" \
    "ds_read_b64_tr_b16 v[242:243], v124 offset:25600\n" \
    "v_mfma_f32_16x16x32_bf16 v[136:139], v[208:211], v[200:203], v[136:139]\n" \
    "ds_read_b128 v[224:227], v121 offset:16384\n" \
    "v_mfma_f32_16x16x32_bf16 v[140:143], v[208:211], v[204:207], v[140:143]\n" \
    "ds_read_b128 v[228:231], v123 offset:16384\n" \
    "v_mfma_f32_16x16x32_bf16 v[144:147], v[212:215], v[192:195], v[144:147]\n" \
    "ds_read_b128 v[232:235], v121 offset:20480\n" \
    "v_mfma_f32_16x16x32_bf16 v[148:151], v[212:215], v[196:199], v[148:151]\n" \
    "ds_read_b128 v[236:239], v123 offset:20480\n" \
    "v_mfma_f32_16x16x32_bf16 v[152:155], v[212:215], v[200:203], v[152:155]\n" \
    "ds_read_b64_tr_b16 v[244:245], v125 offset:24576\n" \
    "v_mfma_f32_16x16x32_bf16 v[156:159], v[212:215], v[204:207], v[156:159]\n" \
    "ds_read_b64_tr_b16 v[246:247], v125 offset:25600\n" \
    "v_mfma_f32_16x16x32_bf16 v[160:163], v[216:219], v[192:195], v[160:163]\n" \
    "ds_read_b64_tr_b16 v[248:249], v126 offset:24576\n" \
    "v_mfma_f32_16x16x32_bf16 v[164:167], v[216:219], v[196:199], v[164:167]\n" \
    "ds_read_b64_tr_b16 v[250:251], v126 offset:25600\n" \
    "v_mfma_f32_16x16x32_bf16 v[168:171], v[216:219], v[200:203], v[168:171]\n" \
    "ds_read_b64_tr_b16 v[252:253], v127 offset:24576\n" \
    "v_mfma_f32_16x16x32_bf16 v[172:175], v[216:219], v[204:207], v[172:175]\n" \
    "ds_read_b64_tr_b16 v[254:255], v127 offset:25600\n" \
    "v_mfma_f32_16x16x32_bf16 v[176:179], v[220:223], v[192:195], v[176:179]\n" \
    "v_add_u32 v124, s61, v124\n" \
    "v_add_u32 v125, s61, v125\n" \
    "v_mfma_f32_16x16x32_bf16 v[180:183], v[220:223], v[196:199], v[180:183]\n" \
    "v_add_u32 v126, s61, v126\n" \
    "v_add_u32 v127, s61, v127\n" \
    "v_mfma_f32_16x16x32_bf16 v[184:187], v[220:223], v[200:203], v[184:187]\n" \
    "v_add_u32 v121, s60, v121\n" \
    "v_add_u32 v123, s60, v123\n" \
    "v_mfma_f32_16x16x32_bf16 v[188:191], v[220:223], v[204:207], v[188:191]\n" \
    "s_waitcnt lgkmcnt(0)\n" \
    "s_cbranch_scc0 82f\n" \
    "s_barrier\n" \
    "82:\n" \
    "v_mfma_f32_16x16x32_bf16 v[128:131], v[240:243], v[224:227], v[128:131]\n" \
    "ds_read_b64_tr_b16 v[208:209], v124 offset:0\n" \
    "v_mfma_f32_16x16x32_bf16 v[132:135], v[240:243], v[228:231], v[132:135]\n" \
    "ds_read_b64_tr_b16 v[210:211], v124 offset:1024\n" \
    "v_mfma_f32_16x16x32_bf16 v[136:139], v[240:243], v[232:235], v[136:139]\n" \
    "ds_read_b128 v[192:195], v120 offset:0\n" \
    "v_mfma_f32_16x16x32_bf16 v[140:143], v[240:243], v[236:239], v[140:143]\n" \
    "ds_read_b128 v[196:199], v122 offset:0\n" \
    "v_mfma_f32_16x16x32_bf16 v[144:147], v[244:247], v[224:227], v[144:147]\n" \
    "ds_read_b128 v[200:203], v120 offset:4096\n" \
    "v_mfma_f32_16x16x32_bf16 v[148:151], v[244:247], v[228:231], v[148:151]\n" \
    "ds_read_b128 v[204:207], v122 offset:4096\n" \
    "v_mfma_f32_16x16x32_bf16 v[152:155], v[244:247], v[232:235], v[152:155]\n" \
    "ds_read_b64_tr_b16 v[212:213], v125 offset:0\n" \
    "v_mfma_f32_16x16x32_bf16 v[156:159], v[244:247], v[236:239], v[156:159]\n" \
    "ds_read_b64_tr_b16 v[214:215], v125 offset:1024\n" \
    "v_mfma_f32_16x16x32_bf16 v[160:163], v[248:251], v[224:227], v[160:163]\n" \
    "ds_read_b64_tr_b16 v[216:217], v126 offset:0\n" \
    "v_mfma_f32_16x16x32_bf16 v[164:167], v[248:251], v[228:231], v[164:167]\n" \
    "ds_read_b64_tr_b16 v[218:219], v126 offset:1024\n" \
    "v_mfma_f32_16x16x32_bf16 v[168:171], v[248:251], v[232:235], v[168:171]\n" \
    "ds_read_b64_tr_b16 v[220:221], v127 offset:0\n" \
    "v_mfma_f32_16x16x32_bf16 v[172:175], v[248:251], v[236:239], v[172:175]\n" \
    "ds_read_b64_tr_b16 v[222:223], v127 offset:1024\n" \
    "s_sub_u32 s61, 0, s61\n" \
    "v_mfma_f32_16x16x32_bf16 v[176:179], v[252:255], v[224:227], v[176:179]\n" \
    "s_add_u32 s58, s58, 1\n" \
    "v_mfma_f32_16x16x32_bf16 v[180:183], v[252:255], v[228:231], v[180:183]\n" \
    "s_cmp_lt_u32 s58, 5\n" \
    "v_mfma_f32_16x16x32_bf16 v[184:187], v[252:255], v[232:235], v[184:187]\n" \
    "v_mfma_f32_16x16x32_bf16 v[188:191], v[252:255], v[236:239], v[188:191]\n" \
    "s_cbranch_scc1 80b\n" \
    "s_branch 99f\n" \
    ".p2align 12\n" \
    ".Lis11: s_cbranch_execz .Lis_ret\n" \
    "95:\n" \
    "s_waitcnt lgkmcnt(0)\n" \
    "v_mfma_f32_16x16x32_bf16 v[128:131], v[192:195], v[208:211], v[128:131]\n" \
    "ds_read_b64_tr_b16 v[240:241], v124 offset:8192\n" \
    "v_mfma_f32_16x16x32_bf16 v[132:135], v[196:199], v[208:211], v[132:135]\n" \
    "ds_read_b64_tr_b16 v[242:243], v124 offset:9216\n" \
    "v_mfma_f32_16x16x32_bf16 v[136:139], v[200:203], v[208:211], v[136:139]\n" \
    "ds_read_b64_tr_b16 v[244:245], v125 offset:8192\n" \
    "v_mfma_f32_16x16x32_bf16 v[140:143], v[204:207], v[208:211], v[140:143]\n" \
    "ds_read_b64_tr_b16 v[246:247], v125 offset:9216\n" \
    "v_mfma_f32_16x16x32_bf16 v[144:147], v[192:195], v[212:215], v[144:147]\n" \
    "ds_read_b64_tr_b16 v[248:249], v126 offset:8192\n" \
    "v_mfma_f32_16x16x32_bf16 v[148:151], v[196:199], v[212:215], v[148:151]\n" \
    "ds_read_b64_tr_b16 v[250:251], v126 offset:9216\n" \
    "v_mfma_f32_16x16x32_bf16 v[152:155], v[200:203], v[212:215], v[152:155]\n" \
    "ds_read_b64_tr_b16 v[252:253], v127 offset:8192\n" \
    "v_mfma_f32_16x16x32_bf16 v[156:159], v[204:207], v[212:215], v[156:159]\n" \
    "ds_read_b64_tr_b16 v[254:255], v127 offset:9216\n" \
    "v_mfma_f32_16x16x32_bf16 v[160:163], v[192:195], v[216:219], v[160:163]\n" \
    "v_mfma_f32_16x16x32_bf16 v[164:167], v[196:199], v[216:219], v[164:167]\n" \
    "v_mfma_f32_16x16x32_bf16 v[168:171], v[200:203], v[216:219], v[168:171]\n" \
    "v_mfma_f32_16x16x32_bf16 v[172:175], v[204:207], v[216:219], v[172:175]\n" \
    "v_mfma_f32_16x16x32_bf16 v[176:179], v[192:195], v[220:223], v[176:179]\n" \
    "v_mfma_f32_16x16x32_bf16 v[180:183], v[196:199], v[220:223], v[180:183]\n" \
    "v_mfma_f32_16x16x32_bf16 v[184:187], v[200:203], v[220:223], v[184:187]\n" \
    "v_mfma_f32_16x16x32_bf16 v[188:191], v[204:207], v[220:223], v[188:191]\n" \
    "s_waitcnt vmcnt(0) lgkmcnt(0)\n" \
    "s_barrier\n" \
    "v_mfma_f32_16x16x32_bf16 v[128:131], v[224:227], v[240:243], v[128:131]\n" \
    "ds_read_b64_tr_b16 v[208:209], v124 offset:16384\n" \
    "v_mfma_f32_16x16x32_bf16 v[132:135], v[228:231], v[240:243], v[132:135]\n" \
    "ds_read_b64_tr_b16 v[210:211], v124 offset:17408\n" \
    "v_mfma_f32_16x16x32_bf16 v[136:139], v[232:235], v[240:243], v[136:139]\n" \
    "ds_read_b64_tr_b16 v[212:213], v125 offset:16384\n" \
    "v_mfma_f32_16x16x32_bf16 v[140:143], v[236:239], v[240:243], v[140:143]\n" \
    "ds_read_b64_tr_b16 v[214:215], v125 offset:17408\n" \
    "v_mfma_f32_16x16x32_bf16 v[144:147], v[224:227], v[244:247], v[144:147]\n" \
    "ds_read_b64_tr_b16 v[216:217], v126 offset:16384\n" \
    "v_mfma_f32_16x16x32_bf16 v[148:151], v[228:231], v[244:247], v[148:151]\n" \
    "ds_read_b64_tr_b16 v[218:219], v126 offset:17408\n" \
    "v_mfma_f32_16x16x32_bf16 v[152:155], v[232:235], v[244:247], v[152:155]\n" \
    "ds_read_b64_tr_b16 v[220:221], v127 offset:16384\n" \
    "v_mfma_f32_16x16x32_bf16 v[156:159], v[236:239], v[244:247], v[156:159]\n" \
    "ds_read_b64_tr_b16 v[222:223], v127 offset:17408\n" \
    "v_mfma_f32_16x16x32_bf16 v[160:163], v[224:227], v[248:251], v[160:163]\n" \
    "v_mfma_f32_16x16x32_bf16 v[164:167], v[228:231], v[248:251], v[164:167]\n" \
    "v_mfma_f32_16x16x32_bf16 v[168:171], v[232:235], v[248:251], v[168:171]\n" \
    "v_mfma_f32_16x16x32_bf16 v[172:175], v[236:239], v[248:251], v[172:175]\n" \
    "v_mfma_f32_16x16x32_bf16 v[176:179], v[224:227], v[252:255], v[176:179]\n" \
    "v_mfma_f32_16x16x32_bf16 v[180:183], v[228:231], v[252:255], v[180:183]\n" \
    "v_mfma_f32_16x16x32_bf16 v[184:187], v[232:235], v[252:255], v[184:187]\n" \
    "v_mfma_f32_16x16x32_bf16 v[188:191], v[236:239], v[252:255], v[188:191]\n" \
    "s_waitcnt lgkmcnt(0)\n" \
    "v_mfma_f32_16x16x32_bf16 v[128:131], v[0:3], v[208:211], v[128:131]\n" \
    "ds_read_b64_tr_b16 v[240:241], v124 offset:24576\n" \
    "v_mfma_f32_16x16x32_bf16 v[132:135], v[4:7], v[208:211], v[132:135]\n" \
    "ds_read_b64_tr_b16 v[242:243], v124 offset:25600\n" \
    "v_mfma_f32_16x16x32_bf16 v[136:139], v[8:11], v[208:211], v[136:139]\n" \
    "ds_read_b64_tr_b16 v[244:245], v125 offset:24576\n" \
    "v_mfma_f32_16x16x32_bf16 v[140:143], v[12:15], v[208:211], v[140:143]\n" \
    "ds_read_b64_tr_b16 v[246:247], v125 offset:25600\n" \
    "v_mfma_f32_16x16x32_bf16 v[144:147], v[0:3], v[212:215], v[144:147]\n" \
    "ds_read_b64_tr_b16 v[248:249], v126 offset:24576\n" \
    "v_mfma_f32_16x16x32_bf16 v[148:151], v[4:7], v[212:215], v[148:151]\n" \
    "ds_read_b64_tr_b16 v[250:251], v126 offset:25600\n" \
    "v_mfma_f32_16x16x32_bf16 v[152:155], v[8:11], v[212:215], v[152:155]\n" \
    "ds_read_b64_tr_b16 v[252:253], v127 offset:24576\n" \
    "v_mfma_f32_16x16x32_bf16 v[156:159], v[12:15], v[212:215], v[156:159]\n" \
    "ds_read_b64_tr_b16 v[254:255], v127 offset:25600\n" \
    "v_mfma_f32_16x16x32_bf16 v[160:163], v[0:3], v[216:219], v[160:163]\n" \
    "v_mfma_f32_16x16x32_bf16 v[164:167], v[4:7], v[216:219], v[164:167]\n" \
    "v_mfma_f32_16x16x32_bf16 v[168:171], v[8:11], v[216:219], v[168:171]\n" \
    "v_mfma_f32_16x16x32_bf16 v[172:175], v[12:15], v[216:219], v[172:175]\n" \
    "v_mfma_f32_16x16x32_bf16 v[176:179], v[0:3], v[220:223], v[176:179]\n" \
    "v_add_u32 v124, s61, v124\n" \
    "v_add_u32 v125, s61, v125\n" \
    "v_mfma_f32_16x16x32_bf16 v[180:183], v[4:7], v[220:223], v[180:183]\n" \
    "v_add_u32 v126, s61, v126\n" \
    "v_add_u32 v127, s61, v127\n" \
    "v_mfma_f32_16x16x32_bf16 v[184:187], v[8:11], v[220:223], v[184:187]\n" \
    "v_mfma_f32_16x16x32_bf16 v[188:191], v[12:15], v[220:223], v[188:191]\n" \
    "s_waitcnt vmcnt(0) lgkmcnt(0)\n" \
    "v_mfma_f32_16x16x32_bf16 v[128:131], v[16:19], v[240:243], v[128:131]\n" \
    "ds_read_b64_tr_b16 v[208:209], v124 offset:0\n" \
    "v_mfma_f32_16x16x32_bf16 v[132:135], v[20:23], v[240:243], v[132:135]\n" \
    "ds_read_b64_tr_b16 v[210:211], v124 offset:1024\n" \
    "v_mfma_f32_16x16x32_bf16 v[136:139], v[24:27], v[240:243], v[136:139]\n" \
    "ds_read_b64_tr_b16 v[212:213], v125 offset:0\n" \
    "v_mfma_f32_16x16x32_bf16 v[140:143], v[28:31], v[240:243], v[140:143]\n" \
    "ds_read_b64_tr_b16 v[214:215], v125 offset:1024\n" \
    "v_mfma_f32_16x16x32_bf16 v[144:147], v[16:19], v[244:247], v[144:147]\n" \
    "ds_read_b64_tr_b16 v[216:217], v126 offset:0\n" \
    "v_mfma_f32_16x16x32_bf16 v[148:151], v[20:23], v[244:247], v[148:151]\n" \
    "ds_read_b64_tr_b16 v[218:219], v126 offset:1024\n" \
    "v_mfma_f32_16x16x32_bf16 v[152:155], v[24:27], v[244:247], v[152:155]\n" \
    "ds_read_b64_tr_b16 v[220:221], v127 offset:0\n" \
    "v_mfma_f32_16x16x32_bf16 v[156:159], v[28:31], v[244:247], v[156:159]\n" \
    "ds_read_b64_tr_b16 v[222:223], v127 offset:1024\n" \
    "v_mfma_f32_16x16x32_bf16 v[160:163], v[16:19], v[248:251], v[160:163]\n" \
    "v_mfma_f32_16x16x32_bf16 v[164:167], v[20:23], v[248:251], v[164:167]\n" \
    "v_mfma_f32_16x16x32_bf16 v[168:171], v[24:27], v[248:251], v[168:171]\n" \
    "v_mfma_f32_16x16x32_bf16 v[172:175], v[28:31], v[248:251], v[172:175]\n" \
    "v_mfma_f32_16x16x32_bf16 v[176:179], v[16:19], v[252:255], v[176:179]\n" \
    "s_sub_u32 s61, 0, s61\n" \
    "v_mfma_f32_16x16x32_bf16 v[180:183], v[20:23], v[252:255], v[180:183]\n" \
    "v_mfma_f32_16x16x32_bf16 v[184:187], v[24:27], v[252:255], v[184:187]\n" \
    "v_mfma_f32_16x16x32_bf16 v[188:191], v[28:31], v[252:255], v[188:191]\n" \
    "s_waitcnt lgkmcnt(0)\n" \
    "v_mfma_f32_16x16x32_bf16 v[128:131], v[44:47], v[208:211], v[128:131]\n" \
    "ds_read_b64_tr_b16 v[240:241], v124 offset:8192\n" \
    "v_mfma_f32_16x16x32_bf16 v[132:135], v[48:51], v[208:211], v[132:135]\n" \
    "ds_read_b64_tr_b16 v[242:243], v124 offset:9216\n" \
    "v_mfma_f32_16x16x32_bf16 v[136:139], v[52:55], v[208:211], v[136:139]\n" \
    "ds_read_b64_tr_b16 v[244:245], v125 offset:8192\n" \
    "v_mfma_f32_16x16x32_bf16 v[140:143], v[56:59], v[208:211], v[140:143]\n" \
    "ds_read_b64_tr_b16 v[246:247], v125 offset:9216\n" \
    "v_mfma_f32_16x16x32_bf16 v[144:147], v[44:47], v[212:215], v[144:147]\n" \
    "ds_read_b64_tr_b16 v[248:249], v126 offset:8192\n" \
    "v_mfma_f32_16x16x32_bf16 v[148:151], v[48:51], v[212:215], v[148:151]\n" \
    "ds_read_b64_tr_b16 v[250:251], v126 offset:9216\n" \
    "v_mfma_f32_16x16x32_bf16 v[152:155], v[52:55], v[212:215], v[152:155]\n" \
    "ds_read_b64_tr_b16 v[252:253], v127 offset:8192\n" \
    "v_mfma_f32_16x16x32_bf16 v[156:159], v[56:59], v[212:215], v[156:159]\n" \
    "ds_read_b64_tr_b16 v[254:255], v127 offset:9216\n" \
    "v_mfma_f32_16x16x32_bf16 v[160:163], v[44:47], v[216:219], v[160:163]\n" \
    "v_mfma_f32_16x16x32_bf16 v[164:167], v[48:51], v[216:219], v[164:167]\n" \
    "v_mfma_f32_16x16x32_bf16 v[168:171], v[52:55], v[216:219], v[168:171]\n" \
    "v_mfma_f32_16x16x32_bf16 v[172:175], v[56:59], v[216:219], v[172:175]\n" \
    "v_mfma_f32_16x16x32_bf16 v[176:179], v[44:47], v[220:223], v[176:179]\n" \
    "v_mfma_f32_16x16x32_bf16 v[180:183], v[48:51], v[220:223], v[180:183]\n" \
    "v_mfma_f32_16x16x32_bf16 v[184:187], v[52:55], v[220:223], v[184:187]\n" \
    "v_mfma_f32_16x16x32_bf16 v[188:191], v[56:59], v[220:223], v[188:191]\n" \
    "s_waitcnt vmcnt(0) lgkmcnt(0)\n" \
    "s_barrier\n" \
    "v_mfma_f32_16x16x32_bf16 v[128:131], v[60:63], v[240:243], v[128:131]\n" \
    "ds_read_b64_tr_b16 v[208:209], v124 offset:16384\n" \
    "v_mfma_f32_16x16x32_bf16 v[132:135], v[64:67], v[240:243], v[132:135]\n" \
    "ds_read_b64_tr_b16 v[210:211], v124 offset:17408\n" \
    "v_mfma_f32_16x16x32_bf16 v[136:139], v[68:71], v[240:243], v[136:139]\n" \
    "ds_read_b64_tr_b16 v[212:213], v125 offset:16384\n" \
    "v_mfma_f32_16x16x32_bf16 v[140:143], v[72:75], v[240:243], v[140:143]\n" \
    "ds_read_b64_tr_b16 v[214:215], v125 offset:17408\n" \
    "v_mfma_f32_16x16x32_bf16 v[144:147], v[60:63], v[244:247], v[144:147]\n" \
    "ds_read_b64_tr_b16 v[216:217], v126 offset:16384\n" \
    "v_mfma_f32_16x16x32_bf16 v[148:151], v[64:67], v[244:247], v[148:151]\n" \
    "ds_read_b64_tr_b16 v[218:219], v126 offset:17408\n" \
    "v_mfma_f32_16x16x32_bf16 v[152:155], v[68:71], v[244:247], v[152:155]\n" \
    "ds_read_b64_tr_b16 v[220:221], v127 offset:16384\n" \
    "v_mfma_f32_16x16x32_bf16 v[156:159], v[72:75], v[244:247], v[156:159]\n" \
    "ds_read_b64_tr_b16 v[222:223], v127 offset:17408\n" \
    "v_mfma_f32_16x16x32_bf16 v[160:163], v[60:63], v[248:251], v[160:163]\n" \
    "v_mfma_f32_16x16x32_bf16 v[164:167], v[64:67], v[248:251], v[164:167]\n" \
    "v_mfma_f32_16x16x32_bf16 v[168:171], v[68:71], v[248:251], v[168:171]\n" \
    "v_mfma_f32_16x16x32_bf16 v[172:175], v[72:75], v[248:251], v[172:175]\n" \
    "v_mfma_f32_16x16x32_bf16 v[176:179], v[60:63], v[252:255], v[176:179]\n" \
    "v_mfma_f32_16x16x32_bf16 v[180:183], v[64:67], v[252:255], v[180:183]\n" \
    "v_mfma_f32_16x16x32_bf16 v[184:187], v[68:71], v[252:255], v[184:187]\n" \
    ".Lis12: s_cbranch_execz .Lis_ret\n" \
    "v_mfma_f32_16x16x32_bf16 v[188:191], v[72:75], v[252:255], v[188:191]\n" \
    "s_waitcnt lgkmcnt(0)\n" \
    "v_mfma_f32_16x16x32_bf16 v[128:131], v[96:99], v[208:211], v[128:131]\n" \
    "ds_read_b64_tr_b16 v[240:241], v124 offset:24576\n" \
    "v_mfma_f32_16x16x32_bf16 v[132:135], v[100:103], v[208:211], v[132:135]\n" \
    "ds_read_b64_tr_b16 v[242:243], v124 offset:25600\n" \
    "v_mfma_f32_16x16x32_bf16 v[136:139], v[104:107], v[208:211], v[136:139]\n" \
    "ds_read_b64_tr_b16 v[244:245], v125 offset:24576\n" \
    "v_mfma_f32_16x16x32_bf16 v[140:143], v[108:111], v[208:211], v[140:143]\n" \
    "ds_read_b64_tr_b16 v[246:247], v125 offset:25600\n" \
    "v_mfma_f32_16x16x32_bf16 v[144:147], v[96:99], v[212:215], v[144:147]\n" \
    "ds_read_b64_tr_b16 v[248:249], v126 offset:24576\n" \
    "v_mfma_f32_16x16x32_bf16 v[148:151], v[100:103], v[212:215], v[148:151]\n" \
    "ds_read_b64_tr_b16 v[250:251], v126 offset:25600\n" \
    "v_mfma_f32_16x16x32_bf16 v[152:155], v[104:107], v[212:215], v[152:155]\n" \
    "ds_read_b64_tr_b16 v[252:253], v127 offset:24576\n" \
    "v_mfma_f32_16x16x32_bf16 v[156:159], v[108:111], v[212:215], v[156:159]\n" \
    "ds_read_b64_tr_b16 v[254:255], v127 offset:25600\n" \
    "v_mfma_f32_16x16x32_bf16 v[160:163], v[96:99], v[216:219], v[160:163]\n" \
    "v_mfma_f32_16x16x32_bf16 v[164:167], v[100:103], v[216:219], v[164:167]\n" \
    "v_mfma_f32_16x16x32_bf16 v[168:171], v[104:107], v[216:219], v[168:171]\n" \
    "v_mfma_f32_16x16x32_bf16 v[172:175], v[108:111], v[216:219], v[172:175]\n" \
    "v_mfma_f32_16x16x32_bf16 v[176:179], v[96:99], v[220:223], v[176:179]\n" \
    "v_add_u32 v124, s61, v124\n" \
    "v_add_u32 v125, s61, v125\n" \
    "v_mfma_f32_16x16x32_bf16 v[180:183], v[100:103], v[220:223], v[180:183]\n" \
    "v_add_u32 v126, s61, v126\n" \
    "v_add_u32 v127, s61, v127\n" \
    "v_mfma_f32_16x16x32_bf16 v[184:187], v[104:107], v[220:223], v[184:187]\n" \
    "v_mfma_f32_16x16x32_bf16 v[188:191], v[108:111], v[220:223], v[188:191]\n" \
    "s_waitcnt vmcnt(0) lgkmcnt(0)\n" \
    "v_mfma_f32_16x16x32_bf16 v[128:131], v[32:35], v[240:243], v[128:131]\n" \
    "ds_read_b64_tr_b16 v[208:209], v124 offset:0\n" \
    "v_mfma_f32_16x16x32_bf16 v[132:135], v[36:39], v[240:243], v[132:135]\n" \
    "ds_read_b64_tr_b16 v[210:211], v124 offset:1024\n" \
    "v_mfma_f32_16x16x32_bf16 v[136:139], v[76:79], v[240:243], v[136:139]\n" \
    "ds_read_b128 v[192:195], v120 offset:0\n" \
    "v_mfma_f32_16x16x32_bf16 v[140:143], v[80:83], v[240:243], v[140:143]\n" \
    "ds_read_b128 v[196:199], v122 offset:0\n" \
    "v_mfma_f32_16x16x32_bf16 v[144:147], v[32:35], v[244:247], v[144:147]\n" \
    "ds_read_b128 v[200:203], v120 offset:4096\n" \
    "v_mfma_f32_16x16x32_bf16 v[148:151], v[36:39], v[244:247], v[148:151]\n" \
    "ds_read_b128 v[204:207], v122 offset:4096\n" \
    "v_mfma_f32_16x16x32_bf16 v[152:155], v[76:79], v[244:247], v[152:155]\n" \
    "ds_read_b64_tr_b16 v[212:213], v125 offset:0\n" \
    "v_mfma_f32_16x16x32_bf16 v[156:159], v[80:83], v[244:247], v[156:159]\n" \
    "ds_read_b64_tr_b16 v[214:215], v125 offset:1024\n" \
    "v_mfma_f32_16x16x32_bf16 v[160:163], v[32:35], v[248:251], v[160:163]\n" \
    "ds_read_b64_tr_b16 v[216:217], v126 offset:0\n" \
    "v_mfma_f32_16x16x32_bf16 v[164:167], v[36:39], v[248:251], v[164:167]\n" \
    "ds_read_b64_tr_b16 v[218:219], v126 offset:1024\n" \
    "v_mfma_f32_16x16x32_bf16 v[168:171], v[76:79], v[248:251], v[168:171]\n" \
    "ds_read_b64_tr_b16 v[220:221], v127 offset:0\n" \
    "v_mfma_f32_16x16x32_bf16 v[172:175], v[80:83], v[248:251], v[172:175]\n" \
    "ds_read_b64_tr_b16 v[222:223], v127 offset:1024\n" \
    "v_mfma_f32_16x16x32_bf16 v[176:179], v[32:35], v[252:255], v[176:179]\n" \
    "s_sub_u32 s61, 0, s61\n" \
    "v_mfma_f32_16x16x32_bf16 v[180:183], v[36:39], v[252:255], v[180:183]\n" \
    "v_mfma_f32_16x16x32_bf16 v[184:187], v[76:79], v[252:255], v[184:187]\n" \
    "v_mfma_f32_16x16x32_bf16 v[188:191], v[80:83], v[252:255], v[188:191]\n" \
    ".Lis13: s_cbranch_execz .Lis_ret\n" \
    "90:\n" \
    "s_waitcnt lgkmcnt(0)\n" \
    "v_mfma_f32_16x16x32_bf16 v[128:131], v[192:195], v[208:211], v[128:131]\n" \
    "ds_read_b64_tr_b16 v[240:241], v124 offset:8192\n" \
    "v_mfma_f32_16x16x32_bf16 v[132:135], v[196:199], v[208:211], v[132:135]\n" \
    "ds_read_b64_tr_b16 v[242:243], v124 offset:9216\n" \
    "v_mfma_f32_16x16x32_bf16 v[136:139], v[200:203], v[208:211], v[136:139]\n" \
    "ds_read_b128 v[224:227], v121 offset:0\n" \
    "v_mfma_f32_16x16x32_bf16 v[140:143], v[204:207], v[208:211], v[140:143]\n" \
    "ds_read_b128 v[228:231], v123 offset:0\n" \
    "v_mfma_f32_16x16x32_bf16 v[144:147], v[192:195], v[212:215], v[144:147]\n" \
    "ds_read_b128 v[232:235], v121 offset:4096\n" \
    "v_mfma_f32_16x16x32_bf16 v[148:151], v[196:199], v[212:215], v[148:151]\n" \
    "ds_read_b128 v[236:239], v123 offset:4096\n" \
    "v_mfma_f32_16x16x32_bf16 v[152:155], v[200:203], v[212:215], v[152:155]\n" \
    "ds_read_b64_tr_b16 v[244:245], v125 offset:8192\n" \
    "v_mfma_f32_16x16x32_bf16 v[156:159], v[204:207], v[212:215], v[156:159]\n" \
    "ds_read_b64_tr_b16 v[246:247], v125 offset:9216\n" \
    "v_mfma_f32_16x16x32_bf16 v[160:163], v[192:195], v[216:219], v[160:163]\n" \
    "ds_read_b64_tr_b16 v[248:249], v126 offset:8192\n" \
    "v_mfma_f32_16x16x32_bf16 v[164:167], v[196:199], v[216:219], v[164:167]\n" \
    "ds_read_b64_tr_b16 v[250:251], v126 offset:9216\n" \
    "v_mfma_f32_16x16x32_bf16 v[168:171], v[200:203], v[216:219], v[168:171]\n" \
    "ds_read_b64_tr_b16 v[252:253], v127 offset:8192\n" \
    "v_mfma_f32_16x16x32_bf16 v[172:175], v[204:207], v[216:219], v[172:175]\n" \
    "ds_read_b64_tr_b16 v[254:255], v127 offset:9216\n" \
    "v_mfma_f32_16x16x32_bf16 v[176:179], v[192:195], v[220:223], v[176:179]\n" \
    "s_add_u32 s59, s59, 1\n" \
    "s_cmp_eq_u32 s59, 3\n" \
    "v_mfma_f32_16x16x32_bf16 v[180:183], v[196:199], v[220:223], v[180:183]\n" \
    "s_cselect_b32 s60, 0xffff0000, s83\n" \
    "s_cselect_b32 s59, 0, s59\n" \
    "v_mfma_f32_16x16x32_bf16 v[184:187], v[200:203], v[220:223], v[184:187]\n" \
    "s_cmp_lt_u32 s58, s84\n" \
    "v_mfma_f32_16x16x32_bf16 v[188:191], v[204:207], v[220:223], v[188:191]\n" \
    "s_waitcnt lgkmcnt(0)\n" \
    "s_barrier\n" \
    "v_mfma_f32_16x16x32_bf16 v[128:131], v[224:227], v[240:243], v[128:131]\n" \
    "ds_read_b64_tr_b16 v[208:209], v124 offset:16384\n" \
    "v_mfma_f32_16x16x32_bf16 v[132:135], v[228:231], v[240:243], v[132:135]\n" \
    "ds_read_b64_tr_b16 v[210:211], v124 offset:17408\n" \
    "v_mfma_f32_16x16x32_bf16 v[136:139], v[232:235], v[240:243], v[136:139]\n" \
    "ds_read_b128 v[192:195], v120 offset:16384\n" \
    "v_mfma_f32_16x16x32_bf16 v[140:143], v[236:239], v[240:243], v[140:143]\n" \
    "ds_read_b128 v[196:199], v122 offset:16384\n" \
    "v_mfma_f32_16x16x32_bf16 v[144:147], v[224:227], v[244:247], v[144:147]\n" \
    "ds_read_b128 v[200:203], v120 offset:20480\n" \
    "v_mfma_f32_16x16x32_bf16 v[148:151], v[228:231], v[244:247], v[148:151]\n" \
    "ds_read_b128 v[204:207], v122 offset:20480\n" \
    "v_mfma_f32_16x16x32_bf16 v[152:155], v[232:235], v[244:247], v[152:155]\n" \
    "ds_read_b64_tr_b16 v[212:213], v125 offset:16384\n" \
    "v_mfma_f32_16x16x32_bf16 v[156:159], v[236:239], v[244:247], v[156:159]\n" \
    "ds_read_b64_tr_b16 v[214:215], v125 offset:17408\n" \
    "v_mfma_f32_16x16x32_bf16 v[160:163], v[224:227], v[248:251], v[160:163]\n" \
    "ds_read_b64_tr_b16 v[216:217], v126 offset:16384\n" \
    "v_mfma_f32_16x16x32_bf16 v[164:167], v[228:231], v[248:251], v[164:167]\n" \
    "ds_read_b64_tr_b16 v[218:219], v126 offset:17408\n" \
    "v_mfma_f32_16x16x32_bf16 v[168:171], v[232:235], v[248:251], v[168:171]\n" \
    "ds_read_b64_tr_b16 v[220:221], v127 offset:16384\n" \
    "v_mfma_f32_16x16x32_bf16 v[172:175], v[236:239], v[248:251], v[172:175]\n" \
    "ds_read_b64_tr_b16 v[222:223], v127 offset:17408\n" \
    "v_mfma_f32_16x16x32_bf16 v[176:179], v[224:227], v[252:255], v[176:179]\n" \
    "v_add_u32 v120, s60, v120\n" \
    "v_mfma_f32_16x16x32_bf16 v[180:183], v[228:231], v[252:255], v[180:183]\n" \
    "v_add_u32 v122, s60, v122\n" \
    "v_mfma_f32_16x16x32_bf16 v[184:187], v[232:235], v[252:255], v[184:187]\n" \
    "v_mfma_f32_16x16x32_bf16 v[188:191], v[236:239], v[252:255], v[188:191]\n" \
    "s_waitcnt lgkmcnt(0)\n" \
    "v_mfma_f32_16x16x32_bf16 v[128:131], v[192:195], v[208:211], v[128:131]\n" \
    "ds_read_b64_tr_b16 v[240:241], v124 offset:24576\n" \
    "v_mfma_f32_16x16x32_bf16 v[132:135], v[196:199], v[208:211], v[132:135]\n" \
    "ds_read_b64_tr_b16 v[242:243], v124 offset:25600\n" \
    "v_mfma_f32_16x16x32_bf16 v[136:139], v[200:203], v[208:211], v[136:139]\n" \
    "ds_read_b128 v[224:227], v121 offset:16384\n" \
    "v_mfma_f32_16x16x32_bf16 v[140:143], v[204:207], v[208:211], v[140:143]\n" \
    "ds_read_b128 v[228:231], v123 offset:16384\n" \
    "v_mfma_f32_16x16x32_bf16 v[144:147], v[192:195], v[212:215], v[144:147]\n" \
    "ds_read_b128 v[232:235], v121 offset:20480\n" \
    "v_mfma_f32_16x16x32_bf16 v[148:151], v[196:199], v[212:215], v[148:151]\n" \
    "ds_read_b128 v[236:239], v123 offset:20480\n" \
    "v_mfma_f32_16x16x32_bf16 v[152:155], v[200:203], v[212:215], v[152:155]\n" \
    "ds_read_b64_tr_b16 v[244:245], v125 offset:24576\n" \
    "v_mfma_f32_16x16x32_bf16 v[156:159], v[204:207], v[212:215], v[156:159]\n" \
    "ds_read_b64_tr_b16 v[246:247], v125 offset:25600\n" \
    "v_mfma_f32_16x16x32_bf16 v[160:163], v[192:195], v[216:219], v[160:163]\n" \
    "ds_read_b64_tr_b16 v[248:249], v126 offset:24576\n" \
    "v_mfma_f32_16x16x32_bf16 v[164:167], v[196:199], v[216:219], v[164:167]\n" \
    "ds_read_b64_tr_b16 v[250:251], v126 offset:25600\n" \
    "v_mfma_f32_16x16x32_bf16 v[168:171], v[200:203], v[216:219], v[168:171]\n" \
    "ds_read_b64_tr_b16 v[252:253], v127 offset:24576\n" \
    "v_mfma_f32_16x16x32_bf16 v[172:175], v[204:207], v[216:219], v[172:175]\n" \
    "ds_read_b64_tr_b16 v[254:255], v127 offset:25600\n" \
    "v_mfma_f32_16x16x32_bf16 v[176:179], v[192:195], v[220:223], v[176:179]\n" \
    "v_add_u32 v124, s61, v124\n" \
    "v_add_u32 v125, s61, v125\n" \
    "v_mfma_f32_16x16x32_bf16 v[180:183], v[196:199], v[220:223], v[180:183]\n" \
    "v_add_u32 v126, s61, v126\n" \
    "v_add_u32 v127, s61, v127\n" \
    "v_mfma_f32_16x16x32_bf16 v[184:187], v[200:203], v[220:223], v[184:187]\n" \
    "v_add_u32 v121, s60, v121\n" \
    "v_add_u32 v123, s60, v123\n" \
    "v_mfma_f32_16x16x32_bf16 v[188:191], v[204:207], v[220:223], v[188:191]\n" \
    "s_waitcnt lgkmcnt(0)\n" \
    "s_cbranch_scc0 92f\n" \
    "s_barrier\n" \
    "92:\n" \
    "v_mfma_f32_16x16x32_bf16 v[128:131], v[224:227], v[240:243], v[128:131]\n" \
    "ds_read_b64_tr_b16 v[208:209], v124 offset:0\n" \
    "v_mfma_f32_16x16x32_bf16 v[132:135], v[228:231], v[240:243], v[132:135]\n" \
    "ds_read_b64_tr_b16 v[210:211], v124 offset:1024\n" \
    "v_mfma_f32_16x16x32_bf16 v[136:139], v[232:235], v[240:243], v[136:139]\n" \
    "ds_read_b128 v[192:195], v120 offset:0\n" \
    "v_mfma_f32_16x16x32_bf16 v[140:143], v[236:239], v[240:243], v[140:143]\n" \
    "ds_read_b128 v[196:199], v122 offset:0\n" \
    "v_mfma_f32_16x16x32_bf16 v[144:147], v[224:227], v[244:247], v[144:147]\n" \
    "ds_read_b128 v[200:203], v120 offset:4096\n" \
    "v_mfma_f32_16x16x32_bf16 v[148:151], v[228:231], v[244:247], v[148:151]\n" \
    "ds_read_b128 v[204:207], v122 offset:4096\n" \
    "v_mfma_f32_16x16x32_bf16 v[152:155], v[232:235], v[244:247], v[152:155]\n" \
    "ds_read_b64_tr_b16 v[212:213], v125 offset:0\n" \
    "v_mfma_f32_16x16x32_bf16 v[156:159], v[236:239], v[244:247], v[156:159]\n" \
    "ds_read_b64_tr_b16 v[214:215], v125 offset:1024\n" \
    "v_mfma_f32_16x16x32_bf16 v[160:163], v[224:227], v[248:251], v[160:163]\n" \
    "ds_read_b64_tr_b16 v[216:217], v126 offset:0\n" \
    "v_mfma_f32_16x16x32_bf16 v[164:167], v[228:231], v[248:251], v[164:167]\n" \
    "ds_read_b64_tr_b16 v[218:219], v126 offset:1024\n" \
    "v_mfma_f32_16x16x32_bf16 v[168:171], v[232:235], v[248:251], v[168:171]\n" \
    "ds_read_b64_tr_b16 v[220:221], v127 offset:0\n" \
    "v_mfma_f32_16x16x32_bf16 v[172:175], v[236:239], v[248:251], v[172:175]\n" \
    "ds_read_b64_tr_b16 v[222:223], v127 offset:1024\n" \
    "s_sub_u32 s61, 0, s61\n" \
    "v_mfma_f32_16x16x32_bf16 v[176:179], v[224:227], v[252:255], v[176:179]\n" \
    "s_add_u32 s58, s58, 1\n" \
    "v_mfma_f32_16x16x32_bf16 v[180:183], v[228:231], v[252:255], v[180:183]\n" \
    "s_cmp_lt_u32 s58, 5\n" \
    "v_mfma_f32_16x16x32_bf16 v[184:187], v[232:235], v[252:255], v[184:187]\n" \
    "v_mfma_f32_16x16x32_bf16 v[188:191], v[236:239], v[252:255], v[188:191]\n" \
    "s_cbranch_scc1 90b\n" \
    "99:\n" \
    "s_waitcnt lgkmcnt(0)\n" \
    "s_nop 6\n"
#define GEMM_CLOB "v0", "v1", "v2", "v3", "v4", "v5", "v6", "v7", "v8", "v9", "v10", "v11", "v12", "v13", "v14", "v15", "v16", "v17", "v18", "v19", "v20", "v21", "v22", "v23", "v24", "v25", "v26", "v27", "v28", "v29", "v30", "v31", "v32", "v33", "v34", "v35", "v36", "v37", "v38", "v39", "v44", "v45", "v46", "v47", "v48", "v49", "v50", "v51", "v52", "v53", "v54", "v55", "v56", "v57", "v58", "v59", "v60", "v61", "v62", "v63", "v64", "v65", "v66", "v67", "v68", "v69", "v70", "v71", "v72", "v73", "v74", "v75", "v76", "v77", "v78", "v79", "v80", "v81", "v82", "v83", "v96", "v97", "v98", "v99", "v100", "v101", "v102", "v103", "v104", "v105", "v106", "v107", "v108", "v109", "v110", "v111", "v192", "v193", "v194", "v195", "v196", "v197", "v198", "v199", "v200", "v201", "v202", "v203", "v204", "v205", "v206", "v207", "v208", "v209", "v210", "v211", "v212", "v213", "v214", "v215", "v216", "v217", "v218", "v219", "v220", "v221", "v222", "v223", "v224", "v225", "v226", "v227", "v228", "v229", "v230", "v231", "v232", "v233", "v234", "v235", "v236", "v237", "v238", "v239", "v240", "v241", "v242", "v243", "v244", "v245", "v246", "v247", "v248", "v249", "v250", "v251", "v252", "v253", "v254", "v255", "s58", "s59", "s60", "s61", "s75", "s76", "s77", "s83", "s84", "s86", "s87", "s88", "s89"
#define LD_SUBS \
    "s_branch .Lld_skip\n" \
    ".Lld_wx:\n" \
    "s_mov_b32 m0, s74\n" \
    "s_add_u32 s74, s74, 0x4000\n" \
    "global_load_lds_dwordx4 v112, s[68:69]\n" \
    "s_add_u32 m0, m0, 0x400\n" \
    "s_cmp_ge_u32 s74, 0x18000\n" \
    "s_cselect_b32 s75, 0x18000, 0\n" \
    "global_load_lds_dwordx4 v113, s[68:69]\n" \
    "s_add_u32 m0, m0, 0x400\n" \
    "s_sub_u32 s74, s74, s75\n" \
    "global_load_lds_dwordx4 v114, s[68:69]\n" \
    "s_add_u32 m0, m0, 0x400\n" \
    "s_cmp_eq_u32 s72, 9\n" \
    "s_cselect_b32 s75, 0x500, 0\n" \
    "global_load_lds_dwordx4 v115, s[68:69]\n" \
    "s_add_u32 s68, s68, 0x80\n" \
    "s_addc_u32 s69, s69, 0\n" \
    "s_sub_u32 s68, s68, s75\n" \
    "s_subb_u32 s69, s69, 0\n" \
    ".Lld_w:\n" \
    "s_mov_b32 m0, s73\n" \
    "s_add_u32 s73, s73, 0x4000\n" \
    "global_load_lds_dwordx4 v116, s[70:71]\n" \
    "s_add_u32 m0, m0, 0x400\n" \
    "s_cmp_ge_u32 s73, 0x28000\n" \
    "s_cselect_b32 s75, 0x10000, 0\n" \
    "global_load_lds_dwordx4 v117, s[70:71]\n" \
    "s_add_u32 m0, m0, 0x400\n" \
    "s_sub_u32 s73, s73, s75\n" \
    "global_load_lds_dwordx4 v118, s[70:71]\n" \
    "s_add_u32 m0, m0, 0x400\n" \
    "s_add_u32 s72, s72, 1\n" \
    "global_load_lds_dwordx4 v119, s[70:71]\n" \
    ".Lld_set:\n" \
    "s_cmp_ge_u32 s72, 10\n" \
    "s_cselect_b32 s75, 10, 0\n" \
    "s_sub_u32 s72, s72, s75\n" \
    "s_mul_i32 s75, s72, 0x3c000\n" \
    "s_add_u32 s70, s80, s75\n" \
    "s_addc_u32 s71, s81, 0\n" \
    "s_setpc_b64 s[76:77]\n" \
    ".Lhop:\n" \
    "s_getpc_b64 s[86:87]\n" \
    ".Lhop1:\n" \
    "s_add_u32 s86, s86, s82\n" \
    "s_addc_u32 s87, s87, 0\n" \
    "s_add_u32 s86, s86, .Lhop_tab-.Lhop1\n" \
    "s_addc_u32 s87, s87, 0\n" \
    "s_mov_b64 exec, 0\n" \
    "s_setpc_b64 s[86:87]\n" \
    ".Lhop_tab:\n" \
    "s_branch .Lis10\ns_branch .Lis8\ns_branch .Lis9\ns_branch .Lis14\ns_branch .Lis13\ns_branch .Lis11\ns_branch .Lis12\ns_branch .Lis14\ns_branch .Lis15\ns_branch .Lis16\ns_branch .Lis17\ns_branch .Lis0\ns_branch .Lis1\ns_branch .Lis2\ns_branch .Lis3\ns_branch .Lis4\ns_branch .Lis5\ns_branch .Lis6\ns_branch .Lis7\ns_branch .Lis_ret\n" \
    ".Lis_ret:\n" \
    "s_mov_b64 exec, -1\n" \
    "s_add_u32 s82, s82, 16\n" \
    "s_setpc_b64 s[76:77]\n" \
    ".Lld_skip:\n"
#define ACC_ZERO "v_mov_b64 v[128:129], 0\nv_mov_b64 v[130:131], 0\nv_mov_b64 v[132:133], 0\nv_mov_b64 v[134:135], 0\nv_mov_b64 v[136:137], 0\nv_mov_b64 v[138:139], 0\nv_mov_b64 v[140:141], 0\nv_mov_b64 v[142:143], 0\nv_mov_b64 v[144:145], 0\nv_mov_b64 v[146:147], 0\nv_mov_b64 v[148:149], 0\nv_mov_b64 v[150:151], 0\nv_mov_b64 v[152:153], 0\nv_mov_b64 v[154:155], 0\nv_mov_b64 v[156:157], 0\nv_mov_b64 v[158:159], 0\nv_mov_b64 v[160:161], 0\nv_mov_b64 v[162:163], 0\nv_mov_b64 v[164:165], 0\nv_mov_b64 v[166:167], 0\nv_mov_b64 v[168:169], 0\nv_mov_b64 v[170:171], 0\nv_mov_b64 v[172:173], 0\nv_mov_b64 v[174:175], 0\nv_mov_b64 v[176:177], 0\nv_mov_b64 v[178:179], 0\nv_mov_b64 v[180:181], 0\nv_mov_b64 v[182:183], 0\nv_mov_b64 v[184:185], 0\nv_mov_b64 v[186:187], 0\nv_mov_b64 v[188:189], 0\nv_mov_b64 v[190:191], 0\n"
#define ACC_SCALE "v_mul_f32 v128, %2, v128\nv_mul_f32 v129, %2, v129\nv_mul_f32 v130, %2, v130\nv_mul_f32 v131, %2, v131\nv_mul_f32 v132, %2, v132\nv_mul_f32 v133, %2, v133\nv_mul_f32 v134, %2, v134\nv_mul_f32 v135, %2, v135\nv_mul_f32 v136, %2, v136\nv_mul_f32 v137, %2, v137\nv_mul_f32 v138, %2, v138\nv_mul_f32 v139, %2, v139\nv_mul_f32 v140, %2, v140\nv_mul_f32 v141, %2, v141\nv_mul_f32 v142, %2, v142\nv_mul_f32 v143, %2, v143\nv_mul_f32 v144, %2, v144\nv_mul_f32 v145, %2, v145\nv_mul_f32 v146, %2, v146\nv_mul_f32 v147, %2, v147\nv_mul_f32 v148, %2, v148\nv_mul_f32 v149, %2, v149\nv_mul_f32 v150, %2, v150\nv_mul_f32 v151, %2, v151\nv_mul_f32 v152, %2, v152\nv_mul_f32 v153, %2, v153\nv_mul_f32 v154, %2, v154\nv_mul_f32 v155, %2, v155\nv_mul_f32 v156, %2, v156\nv_mul_f32 v157, %2, v157\nv_mul_f32 v158, %2, v158\nv_mul_f32 v159, %2, v159\nv_mul_f32 v160, %2, v160\nv_mul_f32 v161, %2, v161\nv_mul_f32 v162, %2, v162\nv_mul_f32 v163, %2, v163\nv_mul_f32 v164, %2, v164\nv_mul_f32 v165, %2, v165\nv_mul_f32 v166, %2, v166\nv_mul_f32 v167, %2, v167\nv_mul_f32 v168, %2, v168\nv_mul_f32 v169, %2, v169\nv_mul_f32 v170, %2, v170\nv_mul_f32 v171, %2, v171\nv_mul_f32 v172, %2, v172\nv_mul_f32 v173, %2, v173\nv_mul_f32 v174, %2, v174\nv_mul_f32 v175, %2, v175\nv_mul_f32 v176, %2, v176\nv_mul_f32 v177, %2, v177\nv_mul_f32 v178, %2, v178\nv_mul_f32 v179, %2, v179\nv_mul_f32 v180, %2, v180\nv_mul_f32 v181, %2, v181\nv_mul_f32 v182, %2, v182\nv_mul_f32 v183, %2, v183\nv_mul_f32 v184, %2, v184\nv_mul_f32 v185, %2, v185\nv_mul_f32 v186, %2, v186\nv_mul_f32 v187, %2, v187\nv_mul_f32 v188, %2, v188\nv_mul_f32 v189, %2, v189\nv_mul_f32 v190, %2, v190\nv_mul_f32 v191, %2, v191\n"
#define ATT_INIT \
    "s_mov_b32 vcc_lo, 0x100000\n" \
    "s_mov_b32 vcc_hi, 0x100000\n" \
    "v_mov_b32 v241, 0x3f803f80\n" \
    "v_cndmask_b32 v240, 0, v241, vcc\n" \
    "v_cndmask_b32 v241, 0, v241, vcc\n" \
    "v_mov_b64 v[192:193], 0\n" \
    "v_mov_b64 v[194:195], 0\n" \
    "v_mov_b64 v[196:197], 0\n" \
    "v_mov_b64 v[198:199], 0\n" \
    "v_mov_b64 v[200:201], 0\n" \
    "v_mov_b64 v[202:203], 0\n" \
    "v_mov_b64 v[204:205], 0\n" \
    "v_mov_b64 v[206:207], 0\n" \
    "v_mov_b64 v[208:209], 0\n" \
    "v_mov_b64 v[210:211], 0\n" \
    "v_mov_b64 v[212:213], 0\n" \
    "v_mov_b64 v[214:215], 0\n" \
    "v_mov_b64 v[216:217], 0\n" \
    "v_mov_b64 v[218:219], 0\n" \
    "v_mov_b64 v[220:221], 0\n" \
    "v_mov_b64 v[222:223], 0\n" \
    "v_mov_b64 v[242:243], v[240:241]\n" \
    "v_mov_b64 v[244:245], v[240:241]\n" \
    "v_mov_b64 v[246:247], v[240:241]\n" \
    "v_mov_b64 v[248:249], v[240:241]\n" \
    "v_mov_b64 v[250:251], v[240:241]\n" \
    "v_mov_b64 v[252:253], v[240:241]\n" \
    "v_mov_b64 v[254:255], v[240:241]\n" \
    "v_add_u32 v136, %[qd], v85\n" \
    "v_and_b32 v138, 0xff, v85\n" \
    "v_mov_b32 v137, v136\n" \
    "s_cmp_eq_u32 %[grp], 0\n" \
    "s_cbranch_scc1 1f\n" \
    "v_lshl_add_u32 v137, v138, 1, %[qx]\n" \
    "v_sub_u32 v137, v137, v136\n" \
    "v_lshrrev_b32 v0, 8, v92\n" \
    "v_and_b32 v1, 0xff, v92\n" \
    "v_mul_u32_u24 v0, 0x500, v0\n" \
    "v_add_u32 v0, v0, v1\n" \
    "s_branch 2f\n" \
    "1:\n" \
    "v_mov_b64 v[36:37], v[240:241]\n" \
    "v_mov_b64 v[38:39], v[240:241]\n" \
    "v_mov_b64 v[40:41], v[240:241]\n" \
    "v_mov_b64 v[42:43], v[240:241]\n" \
    "v_mov_b64 v[44:45], v[240:241]\n" \
    "v_mov_b64 v[46:47], v[240:241]\n" \
    "v_mov_b64 v[48:49], v[240:241]\n" \
    "v_mov_b64 v[50:51], v[240:241]\n" \
    "v_mov_b64 v[52:53], v[240:241]\n" \
    "v_mov_b64 v[54:55], v[240:241]\n" \
    "v_lshrrev_b32 v3, 1, v92\n" \
    "v_lshrrev_b32 v4, 9, v92\n" \
    "v_mov_b32 v1, v138\n" \
    "v_lshl_add_u32 v0, v4, 10, v138\n" \
    "v_mad_u32_u24 v2, v4, %[pv], v138\n" \
    "v_mul_u32_u24 v4, 0x2800, v4\n" \
    "v_lshl_add_u32 v4, v138, 1, v4\n" \
    "2:\n" \
    "v_add_u32 v138, %[lm], v138\n"
#define ATT_PRO \
    "v_and_b32 v139, 0xffffffef, v94\n" \
    "s_mov_b32 s50, 0x100000\n" \
    "3:\n" \
    "ds_read_b32 v140, v139\n" \
    "s_sub_u32 s50, s50, 1\n" \
    "s_cbranch_scc1 4f\n" \
    "s_waitcnt lgkmcnt(0)\n" \
    "v_cmp_ge_u32 vcc, %[grp], v140\n" \
    "s_nop 0\n" \
    "s_cbranch_vccnz 3b\n" \
    "4:\n" \
    "s_cmp_eq_u32 %[grp], 0\n" \
    "s_cbranch_scc1 1f\n" \
    "ds_read_b64 v[132:133], v138\n" \
    "ds_read_b64 v[232:233], v137 offset:1024\n" \
    "ds_read_b64 v[234:235], v85 offset:1024\n" \
    "ds_read_b64 v[128:129], v85\n" \
    "ds_read_b64 v[130:131], v85 offset:512\n" \
    "ds_read_b64 v[224:225], v136 offset:256\n" \
    "ds_read_b64 v[226:227], v136 offset:768\n" \
    "ds_read_b64 v[228:229], v136 offset:0\n" \
    "ds_read_b64 v[230:231], v136 offset:512\n" \
    "ds_read_b64 v[236:237], v85 offset:256\n" \
    "ds_read_b64 v[238:239], v85 offset:768\n" \
    "s_mov_b64 exec, s[48:49]\n" \
    "ds_read_b128 v[240:243], v86\n" \
    "ds_read_b128 v[244:247], v86 offset:640\n" \
    "s_mov_b64 exec, -1\n" \
    "s_waitcnt lgkmcnt(10)\n" \
    "v_mov_b32 v160, v132\n" \
    "v_mov_b32 v161, v132\n" \
    "v_mov_b32 v176, v133\n" \
    "v_mov_b32 v177, v133\n" \
    "v_mov_b64 v[162:163], v[160:161]\n" \
    "v_mov_b64 v[178:179], v[176:177]\n" \
    "v_mov_b64 v[164:165], v[160:161]\n" \
    "v_mov_b64 v[180:181], v[176:177]\n" \
    "v_mov_b64 v[166:167], v[160:161]\n" \
    "v_mov_b64 v[182:183], v[176:177]\n" \
    "v_mov_b64 v[168:169], v[160:161]\n" \
    "v_mov_b64 v[184:185], v[176:177]\n" \
    "v_mov_b64 v[170:171], v[160:161]\n" \
    "v_mov_b64 v[186:187], v[176:177]\n" \
    "v_mov_b64 v[172:173], v[160:161]\n" \
    "v_mov_b64 v[188:189], v[176:177]\n" \
    "v_mov_b64 v[174:175], v[160:161]\n" \
    "v_mov_b64 v[190:191], v[176:177]\n" \
    "s_nop 1\n" \
    "v_mfma_f32_32x32x4_2b_bf16 v[96:127], v[234:235], v[232:233], v[160:191] cbsz:1\n" \
    "s_waitcnt lgkmcnt(0)\n" \
    "s_nop 15\n" \
    "s_nop 0\n" \
    "v_mfma_f32_32x32x16_bf16 v[96:111], v[128:131], v[224:227], v[96:111]\n" \
    "v_mfma_f32_32x32x16_bf16 v[112:127], v[128:131], v[228:231], v[112:127]\n" \
    "s_nop 11\n" \
    "s_branch 2f\n" \
    "1:\n" \
    "ds_read_b64 v[232:233], v137 offset:1024\n" \
    "ds_read_b64 v[234:235], v85 offset:1024\n" \
    "ds_read_b64 v[128:129], v85\n" \
    "ds_read_b64 v[130:131], v85 offset:512\n" \
    "ds_read_b64 v[224:225], v136 offset:0\n" \
    "ds_read_b64 v[226:227], v136 offset:512\n" \
    "ds_read_b64 v[228:229], v136 offset:256\n" \
    "ds_read_b64 v[230:231], v136 offset:768\n" \
    "ds_read_b64 v[236:237], v85 offset:256\n" \
    "ds_read_b64 v[238:239], v85 offset:768\n" \
    "s_mov_b64 exec, s[48:49]\n" \
    "ds_read_b128 v[240:243], v86\n" \
    "ds_read_b128 v[244:247], v86 offset:640\n" \
    "s_mov_b64 exec, -1\n" \
    "s_waitcnt lgkmcnt(10)\n" \
    "v_mfma_f32_32x32x4_2b_bf16 v[96:127], v[234:235], v[232:233], 0 cbsz:1\n" \
    "s_waitcnt lgkmcnt(0)\n" \
    "s_nop 15\n" \
    "s_nop 0\n" \
    "v_mfma_f32_32x32x16_bf16 v[96:111], v[128:131], v[224:227], v[96:111]\n" \
    "v_mfma_f32_32x32x16_bf16 v[112:127], v[128:131], v[228:231], v[112:127]\n" \
    "s_nop 11\n" \
    "v_max3_f32 v132, v96, v97, v98\n" \
    "v_max3_f32 v132, v132, v99, v100\n" \
    "v_max3_f32 v132, v132, v101, v102\n" \
    "v_max3_f32 v132, v132, v103, v104\n" \
    "v_max3_f32 v132, v132, v105, v106\n" \
    "v_max3_f32 v132, v132, v107, v108\n" \
    "v_max3_f32 v132, v132, v109, v110\n" \
    "v_max_f32 v132, v132, v111\n" \
    "v_max3_f32 v133, v112, v113, v114\n" \
    "v_max3_f32 v133, v133, v115, v116\n" \
    "v_max3_f32 v133, v133, v117, v118\n" \
    "v_max3_f32 v133, v133, v119, v120\n" \
    "v_max3_f32 v133, v133, v121, v122\n" \
    "v_max3_f32 v133, v133, v123, v124\n" \
    "v_max3_f32 v133, v133, v125, v126\n" \
    "v_max_f32 v133, v133, v127\n" \
    "v_mov_b32 v134, v132\n" \
    "v_mov_b32 v135, v133\n" \
    "s_nop 1\n" \
    "v_permlane32_swap_b32 v132, v134\n" \
    "v_permlane32_swap_b32 v133, v135\n" \
    "v_min_f32_e64 v132, -v132, -v134\n" \
    "v_min_f32_e64 v133, -v133, -v135\n" \
    "ds_write2_b32 v138, v133, v132 offset1:1\n" \
    "v_mov_b32 v134, 2\n" \
    "s_cmp_eq_u64 %[miss], 0\n" \
    "s_cselect_b64 exec, -1, 0\n" \
    "s_waitcnt vmcnt(0)\n" \
    "ds_write_b32 v93, v134\n" \
    "s_mov_b64 exec, -1\n" \
    "v_add_f32 v96, v132, v96\n" \
    "v_add_f32 v97, v132, v97\n" \
    "v_add_f32 v98, v132, v98\n" \
    "v_add_f32 v99, v132, v99\n" \
    "v_add_f32 v100, v132, v100\n" \
    "v_add_f32 v101, v132, v101\n" \
    "v_add_f32 v102, v132, v102\n" \
    "v_add_f32 v103, v132, v103\n" \
    "v_add_f32 v104, v132, v104\n" \
    "v_add_f32 v105, v132, v105\n" \
    "v_add_f32 v106, v132, v106\n" \
    "v_add_f32 v107, v132, v107\n" \
    "v_add_f32 v108, v132, v108\n" \
    "v_add_f32 v109, v132, v109\n" \
    "v_add_f32 v110, v132, v110\n" \
    "v_add_f32 v111, v132, v111\n" \
    "v_add_f32 v112, v133, v112\n" \
    "v_add_f32 v113, v133, v113\n" \
    "v_add_f32 v114, v133, v114\n" \
    "v_add_f32 v115, v133, v115\n" \
    "v_add_f32 v116, v133, v116\n" \
    "v_add_f32 v117, v133, v117\n" \
    "v_add_f32 v118, v133, v118\n" \
    "v_add_f32 v119, v133, v119\n" \
    "v_add_f32 v120, v133, v120\n" \
    "v_add_f32 v121, v133, v121\n" \
    "v_add_f32 v122, v133, v122\n" \
    "v_add_f32 v123, v133, v123\n" \
    "v_add_f32 v124, v133, v124\n" \
    "v_add_f32 v125, v133, v125\n" \
    "v_add_f32 v126, v133, v126\n" \
    "v_add_f32 v127, v133, v127\n" \
    "v_mov_b32 v160, v132\n" \
    "v_mov_b32 v161, v132\n" \
    "v_mov_b32 v176, v133\n" \
    "v_mov_b32 v177, v133\n" \
    "v_mov_b64 v[162:163], v[160:161]\n" \
    "v_mov_b64 v[178:179], v[176:177]\n" \
    "v_mov_b64 v[164:165], v[160:161]\n" \
    "v_mov_b64 v[180:181], v[176:177]\n" \
    "v_mov_b64 v[166:167], v[160:161]\n" \
    "v_mov_b64 v[182:183], v[176:177]\n" \
    "v_mov_b64 v[168:169], v[160:161]\n" \
    "v_mov_b64 v[184:185], v[176:177]\n" \
    "v_mov_b64 v[170:171], v[160:161]\n" \
    "v_mov_b64 v[186:187], v[176:177]\n" \
    "v_mov_b64 v[172:173], v[160:161]\n" \
    "v_mov_b64 v[188:189], v[176:177]\n" \
    "v_mov_b64 v[174:175], v[160:161]\n" \
    "v_mov_b64 v[190:191], v[176:177]\n" \
    "2:\n"
static_assert(WS_O == 15728640 && WS_WT == 20971520 && WS_G == 22609920 && WS_F2 == 23035904 && WS_F4 == 23003136 && WS_N == WS_F2 + 32768 && L_S0[0] - L_S1[0] == 57344 && L_S0[1] - L_S1[1] == 57344 && L_WF1[0] - L_S1[0] == 98304 && L_WF1[1] - L_S1[1] == 98304 + 5120 && L_WF1[0] - L_WF2[0] == 108544 && L_WF1[1] - L_WF2[1] == 82944 && WS_F3 == 22740992 && L_WF1[0] == 108544 && L_WF1[1] == 108544 + 25600 && L_WF2[0] == 0 && L_WF2[1] == 51200 && L_S1[0] == 10240 && L_S1[1] == 10240 + 20480, "constants baked into the assembly");
#define ATT_SB "v128", "v129", "v130", "v131", "v132", "v133", "v134", "v135", "v136", "v137", "v138", "v139", "v140", "v141", "v142", "v143", "v144", "v145", "v146", "v147", "v148", "v149", "v150", "v151", "v152", "v153", "v154", "v155", "v156", "v157", "v158", "v159"
#define OUT_AC "v224", "v225", "v226", "v227", "v228", "v229", "v230", "v231", "v232", "v233", "v234", "v235", "v236", "v237", "v238", "v239"
#define OAUX 0
#define WPFN 3
#define WAPF 0
#define ROT (((jj & 7) * 3 + (xc >> 2) * 5 + nt * 3) % 10)
#define E1 2
#define E2 5
#define PUB 14
#define MEMB() asm volatile("" ::: "memory")
#define PAGE() "s_branch 1f\n.p2align 12\n1:\n"
__global__ __launch_bounds__(512, 2) __attribute__((aligned(4096))) void fused_kernel(const char* __restrict__ x, const char* __restrict__ wqa, const char* __restrict__ wqb,
                                                       const char* __restrict__ woa, const char* __restrict__ wob, const char* __restrict__ gw,
                                                       char* ws, char* out) {
    const int b = blockIdx.x, wid = __builtin_amdgcn_readfirstlane(threadIdx.x >> 6);
    const int grp = wid >> 2, qp = wid & 3, xc = b & 7, jj = b >> 3;
    unsigned vlin = (threadIdx.x & 63) * 16;
#define LANEV() unsigned l16_ = vlin; asm volatile("" : "+v"(l16_)); const int lane = (l16_ >> 4) & 63, r = lane & 31, h2 = lane >> 5, r16 = lane & 15, gq = lane >> 4; (void)r; (void)h2; (void)r16; (void)gq
#define FLAG_ST(v, off) do { unsigned lo_ = v[0], hi_ = v[1], l_ = vlin; asm volatile("" : "+s"(lo_), "+s"(hi_), "+v"(l_)); if (l_ == 0) ST64((u32x2{lo_, hi_}), off); } while (0)
    const int head = xc * 4 + (jj & 3), qb = jj >> 2;
    const __amdgpu_buffer_rsrc_t rs = __builtin_amdgcn_make_buffer_rsrc(ws, 0, WS_END, 0x00020000);
    STAMP_DECL(); ESTAMP(0);
    if (wid == 0) { unsigned o = (jj % 11) * 4096 + vlin * 4;
      asm volatile("s_getpc_b64 s[64:65]\n.Lcode_lo:\n\tv_min_u32 %0, .Lcode_hi-.Lcode_lo, %0\n\tglobal_load_dword %0, %0, s[64:65]\n\ts_waitcnt vmcnt(0)" : "+v"(o) :: "s64", "s65"); }
    const int mt = (xc >> 2) * 8 + (jj & 7), nt = (xc & 3) * 4 + (jj >> 3), sec = nt / 5;
    const int rot = ROT;
    u32x8 lo;
    { const unsigned lane = vlin >> 4, r16 = lane & 15, gq = lane >> 4, l3 = lane >> 3;
      const unsigned x0 = (qp * 32 + (l3 ^ (l3 >> 2))) * 1280 + (((lane & 7) ^ ((l3 >> 1) & 1)) << 4), w0 = (qp * 16 + gq) * 3840 + ((r16 ^ (gq << 1)) << 4);
#pragma unroll
      for (int i = 0; i < 4; ++i) { lo[i] = (x0 ^ (i << 5)) + i * 10240; lo[4 + i] = (w0 ^ ((i >> 1) << 7)) + i * 15360; } }
    const char *ld_x = x + mt * (128 * 1280) + rot * 128, *ld_w, *ld_wb = (grp ? wqb : wqa) + nt * 256;
    unsigned ld_k = rot + (grp ? 0 : 3), ld_lx = qp * 4096, ld_lw = (grp ? 98304 : 114688) + ld_lx;
#define LD_CALL(PRE, SUB) asm volatile(PRE "s_call_b64 s[76:77], " SUB : "+{s[68:69]}"(ld_x), "+{s[70:71]}"(ld_w), "+{s72}"(ld_k), "+{s73}"(ld_lw), "+{s74}"(ld_lx) \
        : "{s[80:81]}"(ld_wb), "{v[112:119]}"(lo) : "memory", "scc", "s75", "s76", "s77")
#define STAGE_WX() LD_CALL("", ".Lld_wx")
#define STAGE_W() LD_CALL("", ".Lld_w")
    LD_CALL(LD_SUBS, ".Lld_set");
    if (nt < 15 && grp == 1) STAGE_WX();
    STAMP2(1);
    u64 nonce; asm volatile("s_load_dwordx2 %0, %1, 0x0" : "={s[62:63]}"(nonce) : "s"(ws + WS_N));
    u32x2 tag2;
#define TAG_READY() do { asm volatile("s_waitcnt lgkmcnt(0)" : "+{s[62:63]}"(nonce)); const u64 t_ = nonce + 1; tag2 = u32x2{(unsigned)t_, (unsigned)(t_ >> 32)}; } while (0)
    const unsigned fme = (qb * 32 + head) * 128;
    auto missing = [&](u32x2 f) { return (u64)__builtin_amdgcn_ballot_w64(((f[0] ^ tag2[0]) | (f[1] ^ tag2[1])) != 0); };
    auto ok2 = [&](u32x2 f) { return missing(f) == 0; };
    auto spin = [&](unsigned off, int cap, u64 need = ~0ull) {
        u64 m = need;
#pragma nounroll
        for (int s = 0; s < cap; ++s) { MEMB(); m = missing(LD64(off)); if (!(m & need)) break; __builtin_amdgcn_s_sleep(2); }
        MEMB(); return m; };
    unsigned voff[5], pf = 0;
    { const unsigned lane = vlin >> 4, k0 = head * 2560 + vlin, v0 = SECB + head * 320 + vlin;
      voff[0] = k0; voff[1] = k0 + (lane < 16 ? 2048 : 768);
      const unsigned k2 = k0 + (lane < 16 ? 1792 : 2048), v2 = v0 - 512 + (lane >= 52 ? 9920 : 0); voff[2] = lane < 32 ? k2 : v2;
      voff[3] = v0 + 512 + (((lane + 32) * 205) >> 12) * 9920; voff[4] = v0 + 1536 + (((lane + 96) * 205) >> 12) * 9920;
      asm volatile("" : "+{v87}"(voff[0]), "+{v88}"(voff[1]), "+{v89}"(voff[2]), "+{v90}"(voff[3]), "+{v91}"(voff[4])); }
    const int lp = qb * 4 + qp;
    unsigned p2;
    { LANEV(); const int ta = (lane & 1 ? (lp > 0 ? lp - 1 : 0) : lp) >> 1, na = (r16 < 12 ? r16 >> 2 : 0) * 5 + (lane & 2 ? (head * 20 + 19) >> 7 : (head * 20) >> 7);
      const int tm = gq == 0 ? r16 : ta, tn = gq == 0 ? 15 : na, pb = (((tn & 3) * 8 + (tm & 7)) * 8) | ((tm >> 3) * 4) | (tn >> 2);
      p2 = WS_F2 + (gq < 2 ? pb : b) * 128; asm volatile("" : "+v"(p2)); }
    asm volatile("" : "+{v92}"(vlin));
    const unsigned s0 = grp ? L_S0[1] : L_S0[0], s1 = grp ? L_S1[1] : L_S1[0], wf1 = grp ? L_WF1[1] : L_WF1[0], wf2 = grp ? L_WF2[1] : L_WF2[0];
    const int opair = xc * 4 + (jj >> 3), ocb = jj & 7;
    const unsigned wtA = WS_WT + (ocb * 20 + grp * 10) * 5120;
    int idx = 0xdddd; unsigned vK, vV, vsw = L_PROG + wid * 4, vsr;
    { LANEV(); vK = s0 + h2 * 1280 + r * 8; vV = s0 + 2560 + (h2 * 20 + r) * 16; vsr = L_PROG + ((grp * 4) | (lane & 7)) * 4;
      asm volatile("" : "+{v84}"(idx), "+{v93}"(vsw), "+{v85}"(vK), "+{v86}"(vV), "+{v94}"(vsr)); }
    const char* const kB = ws + WS_T + 4 * SECB;
    auto first_loads = [&](int g) {
        if (g == 0) { const char* q = kB - SECB + (qb * 4 + qp) * PAIRB;
            dma_dev(voff[0], q, L_Q + qp * 2560); dma_dev(voff[1], q, L_Q + qp * 2560 + 1024);
            if (vlin < 512) dma_dev(voff[2], q, L_Q + qp * 2560 + 2048); }
#pragma unroll
        for (int i = 0; i < 5; ++i) dma_dev(voff[i], kB + (g * 16 + qp) * PAIRB, L_S0[g] + qp * 5120 + i * 1024); };
    u32x2 f1; int early = 0; u64 miss = ~0ull; constexpr u64 FIRST = 0xffffffff000000ffull;

    if (grp == 0) { LANEV();
        const unsigned pp = lane < 32 ? (lane >> 3) * 4 + (lane & 4) * 4 + qp : qb * 4 + qp;
        pf = WS_F1 + (pp * 30 + (lane < 32 ? (lane & 2 ? 20 : 10) : 0) + (lane & 1 ? (head * 20 + 19) >> 6 : (head * 20) >> 6)) * 128; asm volatile("" : "+v"(pf)); }
    if (__builtin_expect(nt < 15, 1)) {
        const int wm = qp & 1, wn = qp >> 1;
        unsigned eo;
        u32x8 ga;
        { LANEV();
#pragma unroll
        for (int e = 0; e < 2; ++e) { const int T = sec == 2 ? 8 * (r16 >> 2) + 4 * e + (r16 & 3) : 2 * r16 + e, f = ((T >> 2) & 6) | ((T >> 1) & 1);
            ga[2 * e] = (wm * 64 + (T ^ ((T >> 2) & 1))) * 128 + ((gq ^ f) << 4); ga[2 * e + 1] = ga[2 * e] ^ 64; }
#pragma unroll
        for (int i = 0; i < 4; ++i) { const int q4 = r16 >> 2, key = ((gq & 1) << 2) | q4; ga[4 + i] = 98304 + grp * 32768 + (gq * 8 + q4) * 256 + (((wn * 4 + i) ^ key) << 5) + (lane & 3) * 8; }
          const int c0 = (nt - sec * 5) * 128 + wn * 64;
          const unsigned ev = gq * 10240 + (c0 + r16) * 16, eq = ((c0 >> 2) + gq) * 512 + r16 * 16;
          eo = WS_T + ((1 - grp) * 3 + sec) * SECB + (mt * 2 + wm) * PAIRB + (sec == 2 ? ev : eq); asm volatile("" : "+v"(eo)); }
        f32x32 acc[2];
        asm volatile(ACC_ZERO : "={v[128:159]}"(acc[0]), "={v[160:191]}"(acc[1]), "+{v[120:127]}"(ga));
        unsigned hop = qp * 4;
#define HOP() asm volatile("s_call_b64 s[76:77], .Lhop" : "+{s82}"(hop) :: "scc", "s76", "s77", "s86", "s87")
        asm volatile(GEMM_PROG : "+{v[128:159]}"(acc[0]), "+{v[160:191]}"(acc[1]), "+{v[120:127]}"(ga), "+{s[68:69]}"(ld_x), "+{s[70:71]}"(ld_w), "+{s72}"(ld_k), "+{s73}"(ld_lw), "+{s74}"(ld_lx),
                       "+{s[80:81]}"(ld_wb), "+{s82}"(hop) STAMP_OPS
                     : "{v[112:119]}"(lo), [isv] "s"(nt / 10), [grp] "s"(grp), [wa] "s"(wqa + nt * 256) : "memory", "scc", GEMM_CLOB);
        ESTAMP(3);
        TAG_READY();
        if (grp == 0) STAGE_W(); else *(volatile LAS unsigned*)(size_t)(L_PROG + wid * 4) = 3u;
        asm volatile(".Lis14: s_cbranch_execz .Lis_ret");
        const unsigned sf = sec == 2 ? 256 : 2048, su = sec == 2 ? 40960 : 256;
        if (sec == 0) asm volatile(ACC_SCALE : "+{v[128:159]}"(acc[0]), "+{v[160:191]}"(acc[1]) : "s"(QSCALE));
#pragma unroll
        for (int i = 0; i < 8; ++i) {
            if (i == E1 && grp == 0) { VM_BAR(63); STAMP2(44); STAGE_W(); }
            const f32x32& a = acc[i >> 2]; const int o = (i & 3) * 8;
            __builtin_amdgcn_raw_buffer_store_b128(u32x4{cvtpk(a[o], a[o + 1]), cvtpk(a[o + 2], a[o + 3]), cvtpk(a[o + 4], a[o + 5]), cvtpk(a[o + 6], a[o + 7])}, rs, eo, (i >> 1) * sf + (i & 1) * su, 16);
            SBAR(); asm volatile("s_nop 1"); SBAR();
        }
        STAMP2(4);
        if (grp == 0) { VMB(8 - E1); STAMP2(46); STAGE_W(); STAGE_W(); }
    } else {
        LANEV();
        const int e = mt;
        if (wid == 0 && lane < 8) lds_st<unsigned>(L_PROG + lane * 4, lane < 4 ? 0u : 3u);
#pragma unroll 1
        for (int k = 0; k < 3; ++k) { const int sl = e + k * 16; if (sl < 40) { const int br = sl >= 20, ks = sl - br * 20;
#pragma unroll
            for (int i = 0; i < 5; ++i) { const unsigned L = (wid * 5 + i) * 1024 + lane * 16, row = L / 1280, c = (L - row * 1280) >> 4;
                const unsigned u = ks * 4 + (row >> 3), hp = u / 5, w = u - hp * 5, hi = (row >> 2) & 1;
                const unsigned src = hp * 40 + (w < 4 ? (w >> 1) * 20 + (w & 1) * 4 + hi * 8 : 16 + hi * 20) + (row & 3);
                dma(src * 1280 + ((c ^ ((row & 3) << 2)) << 4), br ? wob : woa, k * 40960 + (k ? 26624 : 0) + (wid * 5 + i) * 1024); } } }
        WAIT_BAR(0);
        TAG_READY();
        STAMP2(2);
#pragma unroll 1
        for (int k = 0; k < 3; ++k) { const int sl = e + k * 16; if (sl < 40) { const int br = sl >= 20, ks = sl - br * 20, q4 = r16 >> 2, p = lane & 3;
#pragma unroll
            for (int n = 0; n < 5; ++n) { const unsigned cbyte = wid * 160 + (n < 4 ? (n >> 1) * 64 + p * 16 + (n & 1) * 8 : 128 + p * 8);
                const unsigned a = k * 40960 + (k ? 26624 : 0) + (gq * 8 + q4) * 1280 + (cbyte ^ (q4 << 6));
                ST128(__builtin_bit_cast(u32x4, cat(ldtr(a), ldtr(a + 4 * 1280))), WS_WT + br * WTB + ((wid * 20 + ks) * 5 + n) * 1024 + lane * 16); } } }
        WAIT_BAR(0);
    }
    const bool ldr = nt < 15 && grp == 0;
    f32x32 SA, SB, negm; u32x16 sc; unsigned s47, bad; f32x16 o0, o1; bfv8 Q0, Q1, Kn; s16x4 Qr, Kr; u32x8 Va, Vb;
    u32x8 za, ofb; f32x32 ofa;
    const char* kvs = kB + (grp * 16 + qp + 4) * PAIRB; unsigned a_s1 = s1 + qp * 5120, a_wa = wtA + qp * 5120;
    unsigned prog = 2; f32x32 wn_; const u64 vmask = 0x000fffff000fffffull;
    asm volatile(PAGE() ".Lis15: s_cbranch_execz .Lis_ret\n\ts_cmp_lg_u32 %1, 0\n\ts_cbranch_scc1 1f\n" LOC_WIN "1:" : "={v[0:31]}"(wn_) : "s"(grp) : "vcc", "scc", "v34");
    if (ldr) {
        asm volatile("s_waitcnt vmcnt(8)" ::: "memory");
        FLAG_ST(tag2, WS_F1 + ((mt * 2 + (qp & 1)) * 30 + nt * 2 + (qp >> 1)) * 128);
        STAMP2(47);
        VM_BAR(1); STAMP2(48); STAGE_W(); STAGE_W();
        if (wid == 3 && vlin < 128) lds_st<unsigned>(L_PROG + (vlin >> 2), 0u);
    }
    {
        u32x4 pa; wn_[5] = __uint_as_float(p2);
        asm volatile(".Lis16: s_cbranch_execz .Lis_ret\n" ATT_INIT : "=&{v[192:207]}"(o0), "=&{v[208:223]}"(o1), "=&{v[240:247]}"(Va), "=&{v[248:255]}"(Vb), "=&{v[36:43]}"(za), "=&{v[44:75]}"(ofa), "=&{v[136:139]}"(pa),
                       "+{v[0:31]}"(wn_), "+{s[42:43]}"(kvs), "+{s44}"(a_s1), "+s"(a_wa)
                     : "s"(gw), "s"(out), "{v85}"(vK), "{v92}"(vlin), [grp] "s"(grp), [qd] "s"(L_Q + qp * 2560 - s0), [qx] "s"(2 * (L_Q + qp * 2560) + 1280), [lm] "s"(L_PROG + 256 + qp * 256),
                       [pv] "s"(lp > 0 ? PAIRB - 256 : 0) : "vcc", "scc");
        if (ldr) {
            STAMP2(49);
            asm volatile("s_waitcnt vmcnt(0)" ::: "memory");
            HLD("dwordx2", f1, pf, ws, 0);
            WAIT_BAR(63); STAMP2(50);
            STAGE_W();
            asm volatile("s_waitcnt vmcnt(4)" : "+v"(f1) :: "memory");
            miss = missing(f1);
            if (miss & FIRST) miss = spin(pf, 1, FIRST); else { first_loads(0); early = 1; }
            if (early) VM_BAR(8); else VM_BAR(0);
        }
        STAMP(5);
        if (grp == 0) {
            if (miss & FIRST) miss = spin(pf, SPIN_MAX, FIRST);
            if (!early) first_loads(0);
            STAMP2(51);
            first_loads(1);
        }
        if (grp == 0) { asm volatile("s_waitcnt vmcnt(5)" ::: "memory"); *(volatile LAS unsigned*)(size_t)vsw = 1u; }
        asm volatile(".Lis17: s_cbranch_execz .Lis_ret");
        asm volatile(ATT_PRO : "=&{v[96:127]}"(SA), "=&{v[160:191]}"(negm), "=&{v[224:227]}"(Q0), "=&{v[228:231]}"(Q1), "=&{v[232:233]}"(Qr), "=&{v[234:235]}"(Kr), "=&{v[236:239]}"(Kn), "+{v[240:247]}"(Va), "+{v[136:139]}"(pa) STAMP_OPS
                     : "{v85}"(vK), "{v86}"(vV), "{s[48:49]}"(vmask), [grp] "s"(grp), "{v93}"(vsw), "{v94}"(vsr), [miss] "s"(miss)
                     : "memory", "scc", "vcc", "s50", "s86", "s87", "s88", "s89", "v128", "v129", "v130", "v131", "v132", "v133", "v134", "v135", "v140");
        if (grp == 0 && miss) { spin(pf, SPIN_MAX); *(volatile LAS unsigned*)(size_t)vsw = 2u; }
    }
    {
        STAMP(7);
        asm volatile(ATT_ALL
            : "+{v[96:127]}"(SA), "+{v[192:207]}"(o0), "+{v[208:223]}"(o1), "+{v[234:235]}"(Kr), "+{v[236:239]}"(Kn), "+{v[240:247]}"(Va), "+{v[248:255]}"(Vb),
              "+{v85}"(vK), "+{v86}"(vV), "+{v94}"(vsr), "+{s[42:43]}"(kvs), "+{s46}"(prog), "+{v[36:43]}"(za), "+{v[44:75]}"(ofa), "=&{v[76:83]}"(ofb), "+{v[0:31]}"(wn_),
              "+{v[160:191]}"(negm), "=&{v[128:159]}"(SB), "=&{s[52:67]}"(sc), "=&{s47}"(s47), "=&{s50}"(bad) STAMP_OPS
            : "{v[224:227]}"(Q0), "{v[228:231]}"(Q1), "{v[232:233]}"(Qr), "{v84}"(idx), "{v93}"(vsw), "{v92}"(vlin), "{s[48:49]}"(vmask),
              "{v87}"(voff[0]), "{v[88:91]}"(u32x4{voff[1], voff[2], voff[3], voff[4]}), "{s44}"(a_s1), "{s[40:41]}"(ws), [tag] "s"(tag2[0] | ((u64)tag2[1] << 32)),
              [b] "s"(b), [wid] "s"(wid), [head] "s"(head), [lp] "s"(lp), [x] "s"(x), [gw] "s"(gw), [wa] "s"(a_wa)
            : "memory", "vcc", "scc", "s45", "s51", "s86", "s87", "s88", "s89", "v34", "v35", "v95");
    }
    if (__builtin_expect(bad != 0, 0)) {
        LANEV();
        float q[20]; float m = -3.0e38f;
#pragma unroll
        for (int g = 0; g < 5; ++g) { const u32x2 v = LD64(WS_T + 3 * SECB + lp * PAIRB + (head * 5 + g) * 512 + (grp * 32 + r) * 8);
            q[4 * g] = __uint_as_float(v[0] << 16); q[4 * g + 1] = __uint_as_float(v[0] & 0xffff0000u); q[4 * g + 2] = __uint_as_float(v[1] << 16); q[4 * g + 3] = __uint_as_float(v[1] & 0xffff0000u); }
#pragma unroll
        for (int i = 0; i < 13; ++i) o0[i] = 0.f;
#pragma unroll 1
        for (int key = 0; key < 2048; ++key) {
            float s = 0.f;
#pragma unroll
            for (int g = 0; g < 5; ++g) { const u32x2 v = LD64(WS_T + 4 * SECB + (key >> 6) * PAIRB + (head * 5 + g) * 512 + (key & 63) * 8);
                s += q[4 * g] * __uint_as_float(v[0] << 16) + q[4 * g + 1] * __uint_as_float(v[0] & 0xffff0000u) + q[4 * g + 2] * __uint_as_float(v[1] << 16) + q[4 * g + 3] * __uint_as_float(v[1] & 0xffff0000u); }
            const float mn = fmaxf(m, s), f = __builtin_amdgcn_exp2f(m - mn), p = __builtin_amdgcn_exp2f(s - mn);
            o0[12] = o0[12] * f + p; m = mn;
#pragma unroll
            for (int i = 0; i < 12; ++i) { const int d = (i & 3) + 8 * (i >> 2) + (i < 8 ? 4 * h2 : 0);
                const unsigned v = LD32(WS_T + 5 * SECB + (key >> 6) * PAIRB + ((key & 63) >> 3) * 10240 + (head * 20 + d) * 16 + (key & 6) * 2);
                o0[i] = o0[i] * f + p * __uint_as_float((key & 1) ? v & 0xffff0000u : v << 16); }
        }
    }
    asm volatile(OUT_ALL ".Lcode_hi:"
        : "+{v[96:127]}"(SA), "+{v[128:159]}"(SB), "+{v[160:191]}"(negm), "+{v[240:247]}"(Va), "+{v[192:207]}"(o0), "+{v[44:75]}"(ofa), "+{v[76:83]}"(ofb), "+{v[0:31]}"(wn_),
          "+{s[52:67]}"(sc), "+{s47}"(s47) STAMP_OPS
        : "{v92}"(vlin), "{s44}"(a_s1), "{s[40:41]}"(ws), [tag] "s"(tag2[0] | ((u64)tag2[1] << 32)), [b] "s"(b), [wid] "s"(wid), [out] "s"(out)
        : "memory", "vcc", "scc", "s50", "s51", "s86", "s87", "s88", "s89", "v34", "v35", "v208", "v209", "v210", "v211", OUT_AC);
    STAMP(18); STAMP_DUMP();
}

extern "C" void kernel_launch(void* const* d_in, const int* in_sizes, int n_in, void* d_out, int out_size, void* d_ws, size_t ws_size, hipStream_t stream) {
    (void)hipFuncSetAttribute((const void*)fused_kernel, hipFuncAttributeMaxDynamicSharedMemorySize, LDS_BYTES);
    fused_kernel<<<256, 512, LDS_BYTES, stream>>>((const char*)d_in[0], (const char*)d_in[1], (const char*)d_in[4], (const char*)d_in[2], (const char*)d_in[5], (const char*)d_in[6],
                                                  (char*)d_ws, (char*)d_out);
}
```
